```python
import torch

def _softmax(x, axis=-1):
    return torch.softmax(x, dim=axis)

def _gelu(x):
    return torch.nn.functional.gelu(x, approximate='tanh')

def _silu(x):
    return torch.nn.functional.silu(x)

def _sigmoid(x):
    return torch.sigmoid(x)

def _layernorm(x, axis=-1, eps=1e-05):
    m = x.mean(dim=axis, keepdim=True)
    v = x.var(dim=axis, keepdim=True, unbiased=False)
    return (x - m) / torch.sqrt(v + eps)

def _scatter_add(tgt, idx, src, axis=0):
    return tgt.clone().index_add_(axis, idx, src)

def _segment_sum(src, seg, n):
    out = torch.zeros((n,) + src.shape[1:], dtype=src.dtype)
    return out.index_add_(0, seg, src)

def _pad(x, pw):
    flat = tuple((v for pair in reversed(pw) for v in pair))
    return torch.nn.functional.pad(x, flat)
kernel_name = 'standalone__tree_speculative_verify_tree_attention_over_candidate_token_tree__decode'

def setup_inputs():

    _w = lambda a: (lambda t: t.to(torch.bfloat16) if t.is_floating_point() else t)(torch.as_tensor(a))
    B, Hq, Hkv, T, C, D = (2, 32, 32, 1, 8192, 192)
    tree_mask = torch.zeros((B, T, T), dtype=float)
    for b in range(B):
        parent = torch.zeros(T, dtype=torch.int64)
        for i in range(1, T):
            parent[i] = int(torch.randint(0, i, (1,)).item())
        for i in range(T):
            j = i
            tree_mask[b, i, j] = 1.0
            while j > 0:
                j = parent[j]
                tree_mask[b, i, j] = 1.0
    return {'q': _w((lambda *s: torch.randn(*s, dtype=torch.bfloat16) * 0.8)(B, Hq, T, D)), 'k_cache': _w((lambda *s: torch.randn(*s, dtype=torch.bfloat16) * 0.8)(B, Hkv, C, D)), 'v_cache': _w((lambda *s: torch.randn(*s, dtype=torch.bfloat16) * 0.05)(B, Hkv, C, D)), 'k_tree': _w((lambda *s: torch.randn(*s, dtype=torch.bfloat16) * 0.8)(B, Hkv, T, D)), 'v_tree': _w((lambda *s: torch.randn(*s, dtype=torch.bfloat16) * 0.05)(B, Hkv, T, D)), 'tree_mask': _w(tree_mask)}

def reference(q, k_cache, v_cache, k_tree, v_tree, tree_mask):
    B, Hq, T, D = q.shape
    Hkv, C = (k_cache.shape[1], k_cache.shape[2])
    rep = Hq // Hkv
    kc = (lambda x, n, axis: torch.repeat_interleave(x, n, dim=axis))(k_cache, rep, axis=1)
    vc = (lambda x, n, axis: torch.repeat_interleave(x, n, dim=axis))(v_cache, rep, axis=1)
    kt = (lambda x, n, axis: torch.repeat_interleave(x, n, dim=axis))(k_tree, rep, axis=1)
    vt = (lambda x, n, axis: torch.repeat_interleave(x, n, dim=axis))(v_tree, rep, axis=1)
    k = torch.concatenate([kc, kt], axis=2)
    v = torch.concatenate([vc, vt], axis=2)
    scale = D ** (-0.5)
    scores = torch.einsum('bhqd,bhkd->bhqk', q, k) * scale
    ctx_mask = torch.ones((B, 1, T, C), dtype=scores.dtype)
    tm = tree_mask[:, None, :, :].clone()
    full_mask = torch.concatenate([ctx_mask, tm], axis=-1)
    scores = scores + (1.0 - full_mask) * -1000000000.0
    p = _softmax(scores, axis=-1)
    return torch.einsum('bhqk,bhkd->bhqd', p, v)
```

```cpp
#include <hip/hip_runtime.h>
#include <hip/hip_bf16.h>
#include <stdint.h>

#define NSPLIT 8
#define WAVES 4
#define UNR 2

namespace {
constexpr int B_ = 2, H_ = 32, C_ = 8192, D_ = 192;
constexpr int BH = B_ * H_;
constexpr int ROWB = D_ * 2;
constexpr int KEYS_PER_WG = C_ / NSPLIT;
constexpr int KEYS_PER_IT = 8 * UNR;
constexpr int NIT = KEYS_PER_WG / (WAVES * KEYS_PER_IT);
static_assert(KEYS_PER_WG % (WAVES * KEYS_PER_IT) == 0, "");
constexpr float SCALE_LOG2E = 0.07216878364870322f * 1.4426950408889634f;
constexpr int PREC = 196;

typedef unsigned u32x4 __attribute__((ext_vector_type(4)));
typedef __bf16 bf16x2 __attribute__((ext_vector_type(2)));
typedef float f32x4 __attribute__((ext_vector_type(4)));

__device__ __forceinline__ float dot2(unsigned a, unsigned b, float c) {
  return __builtin_amdgcn_fdot2_f32_bf16(__builtin_bit_cast(bf16x2, a), __builtin_bit_cast(bf16x2, b), c, false);
}
template <int CTRL> __device__ __forceinline__ float dpp(float x) {
  return __builtin_bit_cast(float, __builtin_amdgcn_mov_dpp(__builtin_bit_cast(int, x), CTRL, 0xf, 0xf, true));
}
constexpr int XOR1 = 0xB1, XOR2 = 0x4E, XOR7 = 0x141, XOR8 = 0x128;
__device__ __forceinline__ float sum8(float x) {
  x += dpp<XOR1>(x); x += dpp<XOR2>(x); x += dpp<XOR7>(x); return x;
}
__device__ __forceinline__ float xsum(float x) {
  x += dpp<XOR8>(x);
  auto s = __builtin_amdgcn_permlane16_swap(__float_as_uint(x), __float_as_uint(x), false, false);
  x = __uint_as_float(s[0]) + __uint_as_float(s[1]);
  auto t = __builtin_amdgcn_permlane32_swap(__float_as_uint(x), __float_as_uint(x), false, false);
  return __uint_as_float(t[0]) + __uint_as_float(t[1]);
}
__device__ __forceinline__ float xmax(float x) {
  x = fmaxf(x, dpp<XOR8>(x));
  auto s = __builtin_amdgcn_permlane16_swap(__float_as_uint(x), __float_as_uint(x), false, false);
  x = fmaxf(__uint_as_float(s[0]), __uint_as_float(s[1]));
  auto t = __builtin_amdgcn_permlane32_swap(__float_as_uint(x), __float_as_uint(x), false, false);
  return fmaxf(__uint_as_float(t[0]), __uint_as_float(t[1]));
}
__device__ __forceinline__ float bflo(unsigned u) { return __uint_as_float(u << 16); }
__device__ __forceinline__ float bfhi(unsigned u) { return __uint_as_float(u & 0xffff0000u); }
__device__ __forceinline__ u32x4 ldnt(const unsigned char* p) { return __builtin_nontemporal_load(reinterpret_cast<const u32x4*>(p)); }

__global__ void __launch_bounds__(WAVES * 64) attn_split(const unsigned char* __restrict__ q, const unsigned char* __restrict__ kc,
                                                         const unsigned char* __restrict__ vc, float* __restrict__ part) {
  __shared__ __attribute__((aligned(16))) float lds[WAVES * PREC];
  const int s = blockIdx.x % NSPLIT, bh = blockIdx.x / NSPLIT;
  const int tid = threadIdx.x, lane = tid & 63, wave = __builtin_amdgcn_readfirstlane(tid >> 6);
  const int r = lane >> 3, c = lane & 7;
  const size_t base = ((size_t)bh * C_ + (size_t)s * KEYS_PER_WG + (size_t)wave * KEYS_PER_IT) * ROWB + r * ROWB + c * 16;
  const unsigned char* kp = kc + base;
  const unsigned char* vp = vc + base;
  constexpr int STEP = WAVES * KEYS_PER_IT * ROWB;
  u32x4 qv[3];
#pragma unroll
  for (int j = 0; j < 3; ++j) qv[j] = *reinterpret_cast<const u32x4*>(q + (size_t)bh * ROWB + j * 128 + c * 16);
  u32x4 kn[UNR][3], vn[UNR][3];
#pragma unroll
  for (int u = 0; u < UNR; ++u)
#pragma unroll
    for (int j = 0; j < 3; ++j) kn[u][j] = ldnt(kp + u * 8 * ROWB + j * 128);
#pragma unroll
  for (int u = 0; u < UNR; ++u)
#pragma unroll
    for (int j = 0; j < 3; ++j) vn[u][j] = ldnt(vp + u * 8 * ROWB + j * 128);
  float m = -INFINITY, l = 0.f, acc[24];
#pragma unroll
  for (int k = 0; k < 24; ++k) acc[k] = 0.f;
  for (int it = 0; it < NIT; ++it) {
    u32x4 kcur[UNR][3], vcur[UNR][3];
#pragma unroll
    for (int u = 0; u < UNR; ++u)
#pragma unroll
      for (int j = 0; j < 3; ++j) { kcur[u][j] = kn[u][j]; vcur[u][j] = vn[u][j]; }
    if (it + 1 < NIT) {
      kp += STEP; vp += STEP;
#pragma unroll
      for (int u = 0; u < UNR; ++u)
#pragma unroll
        for (int j = 0; j < 3; ++j) kn[u][j] = ldnt(kp + u * 8 * ROWB + j * 128);
#pragma unroll
      for (int u = 0; u < UNR; ++u)
#pragma unroll
        for (int j = 0; j < 3; ++j) vn[u][j] = ldnt(vp + u * 8 * ROWB + j * 128);
    }
    float sc[UNR];
    float mn = m;
#pragma unroll
    for (int u = 0; u < UNR; ++u) {
      float d0 = 0.f, d1 = 0.f, d2 = 0.f;
#pragma unroll
      for (int k = 0; k < 4; ++k) { d0 = dot2(kcur[u][0][k], qv[0][k], d0); d1 = dot2(kcur[u][1][k], qv[1][k], d1); d2 = dot2(kcur[u][2][k], qv[2][k], d2); }
      sc[u] = sum8((d0 + d1) + d2) * SCALE_LOG2E;
      mn = fmaxf(mn, sc[u]);
    }
    const float alpha = __builtin_amdgcn_exp2f(m - mn);
    m = mn;
    l *= alpha;
#pragma unroll
    for (int k = 0; k < 24; ++k) acc[k] *= alpha;
#pragma unroll
    for (int u = 0; u < UNR; ++u) {
      const float p = __builtin_amdgcn_exp2f(sc[u] - mn);
      l += p;
#pragma unroll
      for (int j = 0; j < 3; ++j)
#pragma unroll
        for (int k = 0; k < 4; ++k) {
          acc[j * 8 + 2 * k] = fmaf(p, bflo(vcur[u][j][k]), acc[j * 8 + 2 * k]);
          acc[j * 8 + 2 * k + 1] = fmaf(p, bfhi(vcur[u][j][k]), acc[j * 8 + 2 * k + 1]);
        }
    }
  }
  const float M = xmax(m);
  const float w = __builtin_amdgcn_exp2f(m - M);
  l = xsum(l * w);
#pragma unroll
  for (int k = 0; k < 24; ++k) acc[k] = xsum(acc[k] * w);
  float* rec = lds + wave * PREC;
  if (r == 0) {
#pragma unroll
    for (int j = 0; j < 3; ++j) {
      *reinterpret_cast<f32x4*>(rec + 4 + j * 64 + c * 8) = f32x4{acc[j * 8], acc[j * 8 + 1], acc[j * 8 + 2], acc[j * 8 + 3]};
      *reinterpret_cast<f32x4*>(rec + 4 + j * 64 + c * 8 + 4) = f32x4{acc[j * 8 + 4], acc[j * 8 + 5], acc[j * 8 + 6], acc[j * 8 + 7]};
    }
    if (c == 0) { rec[0] = M; rec[1] = l; }
  }
  __syncthreads();
  if (tid < D_) {
    float MM = lds[0];
#pragma unroll
    for (int w2 = 1; w2 < WAVES; ++w2) MM = fmaxf(MM, lds[w2 * PREC]);
    float L = 0.f, o = 0.f;
#pragma unroll
    for (int w2 = 0; w2 < WAVES; ++w2) {
      const float wt = __builtin_amdgcn_exp2f(lds[w2 * PREC] - MM);
      L = fmaf(wt, lds[w2 * PREC + 1], L);
      o = fmaf(wt, lds[w2 * PREC + 4 + tid], o);
    }
    float* pr = part + (size_t)blockIdx.x * PREC;
    pr[4 + tid] = o;
    if (tid == 0) { pr[0] = MM; pr[1] = L; }
  }
}

__global__ void __launch_bounds__(192) attn_combine(const unsigned short* __restrict__ q, const unsigned short* __restrict__ kt,
                                                    const unsigned short* __restrict__ vt, const unsigned short* __restrict__ tm,
                                                    const float* __restrict__ part, __hip_bfloat16* __restrict__ out) {
  __shared__ float red[4];
  const int bh = blockIdx.x, tid = threadIdx.x;
  const float* pr = part + (size_t)bh * NSPLIT * PREC;
  float ms[NSPLIT], ls[NSPLIT], os[NSPLIT];
#pragma unroll
  for (int s = 0; s < NSPLIT; ++s) { ms[s] = pr[s * PREC]; ls[s] = pr[s * PREC + 1]; os[s] = pr[s * PREC + 4 + tid]; }
  const float qf = __uint_as_float((unsigned)q[bh * D_ + tid] << 16), kf = __uint_as_float((unsigned)kt[bh * D_ + tid] << 16);
  const float vf = __uint_as_float((unsigned)vt[bh * D_ + tid] << 16);
  const float mk = __uint_as_float((unsigned)tm[bh / H_] << 16);
  float d = xsum(sum8(qf * kf));
  if ((tid & 63) == 0) red[tid >> 6] = d;
  __syncthreads();
  const float st = (red[0] + red[1] + red[2]) * SCALE_LOG2E + (1.0f - mk) * (-1.0e9f * 1.4426950408889634f);
  float M = st;
#pragma unroll
  for (int s = 0; s < NSPLIT; ++s) M = fmaxf(M, ms[s]);
  const float wt = __builtin_amdgcn_exp2f(st - M);
  float L = wt, o = wt * vf;
#pragma unroll
  for (int s = 0; s < NSPLIT; ++s) { const float w = __builtin_amdgcn_exp2f(ms[s] - M); L = fmaf(w, ls[s], L); o = fmaf(w, os[s], o); }
  out[bh * D_ + tid] = __float2bfloat16(o / L);
}
}

extern "C" void kernel_launch(void* const* d_in, const int* in_sizes, int n_in, void* d_out, int out_size, void* d_ws, size_t ws_size,
                              hipStream_t stream) {
  float* part = (float*)d_ws;
  attn_split<<<BH * NSPLIT, WAVES * 64, 0, stream>>>((const unsigned char*)d_in[0], (const unsigned char*)d_in[1], (const unsigned char*)d_in[2], part);
  attn_combine<<<BH, 192, 0, stream>>>((const unsigned short*)d_in[0], (const unsigned short*)d_in[3], (const unsigned short*)d_in[4],
                                       (const unsigned short*)d_in[5], part, (__hip_bfloat16*)d_out);
}
```

```cpp
#include <hip/hip_runtime.h>
#include <hip/hip_bf16.h>
#include <stdint.h>

namespace ta {
constexpr int B_ = 2, H_ = 32, C_ = 8192, D_ = 192;
constexpr int BH = B_ * H_;
constexpr int ROWB = D_ * 2;
constexpr int TILEB = 8 * ROWB;
constexpr int HEADB = C_ * ROWB;
constexpr int NTILE = C_ / 8;
constexpr float LOG2E = 1.4426950408889634f;
constexpr float SCALE_LOG2E = 0.07216878364870322f * LOG2E;
constexpr int PREC = 196;
constexpr int WS_HEAD = 20736;
constexpr int WS_SLOT0 = 1024, WS_SLOT = 1792;

#define GAS __attribute__((address_space(1)))
typedef unsigned u32x4 __attribute__((ext_vector_type(4)));
typedef __bf16 bf16x2 __attribute__((ext_vector_type(2)));
typedef float f32x4 __attribute__((ext_vector_type(4)));
typedef float f32x2 __attribute__((ext_vector_type(2)));
typedef GAS unsigned gu32;
typedef GAS unsigned long long gu64;
#define RLX_AGENT __ATOMIC_RELAXED, __HIP_MEMORY_SCOPE_AGENT

__device__ __forceinline__ float dot2(unsigned a, unsigned b, float c) {
  return __builtin_amdgcn_fdot2_f32_bf16(__builtin_bit_cast(bf16x2, a), __builtin_bit_cast(bf16x2, b), c, false);
}
template <int CTRL> __device__ __forceinline__ float dpp(float x) {
  return __builtin_bit_cast(float, __builtin_amdgcn_mov_dpp(__builtin_bit_cast(int, x), CTRL, 0xf, 0xf, true));
}
constexpr int XOR1 = 0xB1, XOR2 = 0x4E, XOR7 = 0x141, XOR8 = 0x128;
__device__ __forceinline__ float sum8(float x) {
  x += dpp<XOR1>(x); x += dpp<XOR2>(x); x += dpp<XOR7>(x); return x;
}
__device__ __forceinline__ float xsum(float x) {
  x += dpp<XOR8>(x);
  auto s = __builtin_amdgcn_permlane16_swap(__float_as_uint(x), __float_as_uint(x), false, false);
  x = __uint_as_float(s[0]) + __uint_as_float(s[1]);
  auto t = __builtin_amdgcn_permlane32_swap(__float_as_uint(x), __float_as_uint(x), false, false);
  return __uint_as_float(t[0]) + __uint_as_float(t[1]);
}
__device__ __forceinline__ float xmax(float x) {
  x = fmaxf(x, dpp<XOR8>(x));
  auto s = __builtin_amdgcn_permlane16_swap(__float_as_uint(x), __float_as_uint(x), false, false);
  x = fmaxf(__uint_as_float(s[0]), __uint_as_float(s[1]));
  auto t = __builtin_amdgcn_permlane32_swap(__float_as_uint(x), __float_as_uint(x), false, false);
  return fmaxf(__uint_as_float(t[0]), __uint_as_float(t[1]));
}
__device__ __forceinline__ float bflo(unsigned u) { return __uint_as_float(u << 16); }
__device__ __forceinline__ float bfhi(unsigned u) { return __uint_as_float(u & 0xffff0000u); }
__device__ __forceinline__ float ex2(float x) { return __builtin_amdgcn_exp2f(x); }

struct State { float m, l; f32x4 a[6]; };

template <int T> struct Buf { u32x4 k[T][3], v[T][3]; };

template <int T>
__device__ __forceinline__ void load_tiles(Buf<T>& b, __amdgpu_buffer_rsrc_t ks, __amdgpu_buffer_rsrc_t vs, unsigned loff, unsigned so) {
#pragma unroll
  for (int t = 0; t < T; ++t)
#pragma unroll
    for (int j = 0; j < 3; ++j) b.k[t][j] = __builtin_amdgcn_raw_buffer_load_b128(ks, loff + t * TILEB + j * 128, so, 2);
#pragma unroll
  for (int t = 0; t < T; ++t)
#pragma unroll
    for (int j = 0; j < 3; ++j) b.v[t][j] = __builtin_amdgcn_raw_buffer_load_b128(vs, loff + t * TILEB + j * 128, so, 2);
  asm volatile("" ::: "memory");
  __builtin_amdgcn_sched_barrier(0);
}

template <int T>
__device__ __forceinline__ void compute(State& st, const Buf<T>& b, const u32x4 (&qv)[3]) {
  float sc[T];
  float mn = st.m;
#pragma unroll
  for (int t = 0; t < T; ++t) {
    float d0 = 0.f, d1 = 0.f, d2 = 0.f;
#pragma unroll
    for (int k = 0; k < 4; ++k) { d0 = dot2(b.k[t][0][k], qv[0][k], d0); d1 = dot2(b.k[t][1][k], qv[1][k], d1); d2 = dot2(b.k[t][2][k], qv[2][k], d2); }
    sc[t] = sum8((d0 + d1) + d2) * SCALE_LOG2E;
    mn = fmaxf(mn, sc[t]);
  }
  const float alpha = ex2(st.m - mn);
  st.m = mn;
  float l = st.l * alpha;
#pragma unroll
  for (int k = 0; k < 6; ++k) st.a[k] *= alpha;
#pragma unroll
  for (int t = 0; t < T; ++t) {
    const float p = ex2(sc[t] - mn);
    l += p;
#pragma unroll
    for (int j = 0; j < 3; ++j) {
      const f32x4 lo = {bflo(b.v[t][j][0]), bfhi(b.v[t][j][0]), bflo(b.v[t][j][1]), bfhi(b.v[t][j][1])};
      const f32x4 hi = {bflo(b.v[t][j][2]), bfhi(b.v[t][j][2]), bflo(b.v[t][j][3]), bfhi(b.v[t][j][3])};
      st.a[2 * j] = __builtin_elementwise_fma(f32x4{p, p, p, p}, lo, st.a[2 * j]);
      st.a[2 * j + 1] = __builtin_elementwise_fma(f32x4{p, p, p, p}, hi, st.a[2 * j + 1]);
    }
  }
  st.l = l;
  asm volatile("" : "+v"(st.m), "+v"(st.l), "+v"(st.a[0]), "+v"(st.a[1]), "+v"(st.a[2]), "+v"(st.a[3]), "+v"(st.a[4]), "+v"(st.a[5]) :: "memory");
  __builtin_amdgcn_sched_barrier(0);
}

template <int NS, int W, int T, int MODE, int NSTAT, int ILV, int DBG>
__global__ void __launch_bounds__(W * 64) attn_main(const unsigned char* __restrict__ q, const unsigned char* __restrict__ kc,
                                                    const unsigned char* __restrict__ vc, const unsigned char* __restrict__ kt,
                                                    const unsigned char* __restrict__ vt, const unsigned short* __restrict__ tm,
                                                    unsigned short* __restrict__ out, unsigned char* __restrict__ ws) {
  __shared__ __attribute__((aligned(16))) float lds[W * PREC];
  unsigned long long ts[6] = {0, 0, 0, 0, 0, 0};
#define STAMP(i) do { if constexpr (DBG) ts[i] = __builtin_amdgcn_s_memrealtime(); } while (0)
#define DUMP() do { if constexpr (DBG) { if ((threadIdx.x & 63) == 0) { unsigned long long* sp = (unsigned long long*)(ws + (64 << 20)) + (size_t)(blockIdx.x * W + (threadIdx.x >> 6)) * 8; \
    for (int i_ = 0; i_ < 6; ++i_) sp[i_] = ts[i_]; sp[6] = (unsigned)__builtin_amdgcn_s_getreg(20 | (3 << 11)) & 0xfu; sp[7] = nit_; } } } while (0)
  int nit_ = 0;
  STAMP(0);
  const int s = blockIdx.x % NS, bh = blockIdx.x / NS;
  const int tid = threadIdx.x, lane = tid & 63, wave = __builtin_amdgcn_readfirstlane(tid >> 6);
  const int r = lane >> 3, c = lane & 7;
  const unsigned loff = r * ROWB + c * 16;
  const auto ks = __builtin_amdgcn_make_buffer_rsrc((void*)(kc + (size_t)bh * HEADB), 0, HEADB, 0x00020000);
  const auto vs = __builtin_amdgcn_make_buffer_rsrc((void*)(vc + (size_t)bh * HEADB), 0, HEADB, 0x00020000);
  unsigned char* wsh = ws + (size_t)bh * WS_HEAD;
  constexpr int NSTEP_ALL = NTILE / (NS * W * 2 * T);
  constexpr int NST = (MODE == 2) ? NSTAT : NSTEP_ALL;
  constexpr unsigned STEPB = (ILV == 0) ? W * T * TILEB : NS * W * T * TILEB;
  unsigned so = (ILV == 0) ? (unsigned)(s * (NST * 2 * W * T) + wave * T) * TILEB : (unsigned)(s * W + wave) * T * TILEB;
  Buf<T> A, Bf;
  load_tiles<T>(A, ks, vs, loff, so);
  so += STEPB;
  u32x4 qv[3], ktv[3], vtv[3];
#pragma unroll
  for (int j = 0; j < 3; ++j) qv[j] = *reinterpret_cast<const u32x4*>(q + bh * ROWB + j * 128 + c * 16);
#pragma unroll
  for (int j = 0; j < 3; ++j) ktv[j] = *reinterpret_cast<const u32x4*>(kt + bh * ROWB + j * 128 + c * 16);
#pragma unroll
  for (int j = 0; j < 3; ++j) vtv[j] = *reinterpret_cast<const u32x4*>(vt + bh * ROWB + j * 128 + c * 16);
  const unsigned mk = tm[bh / H_];
  unsigned tag = 0;
  if constexpr (MODE >= 1) tag = __hip_atomic_load((gu32*)wsh, RLX_AGENT) * 0x9E3779B1u + 0x7F4A7C15u;
  asm volatile("" ::: "memory");
  __builtin_amdgcn_sched_barrier(0);
  load_tiles<T>(Bf, ks, vs, loff, so);
  so += STEPB;
  State st;
  {
    float d0 = 0.f, d1 = 0.f, d2 = 0.f;
#pragma unroll
    for (int k = 0; k < 4; ++k) { d0 = dot2(ktv[0][k], qv[0][k], d0); d1 = dot2(ktv[1][k], qv[1][k], d1); d2 = dot2(ktv[2][k], qv[2][k], d2); }
    const float sct = sum8((d0 + d1) + d2) * SCALE_LOG2E + (1.0f - bflo(mk)) * (-1.0e9f * LOG2E);
    const bool own = (s == 0) && (tid < 8);
    st.m = own ? sct : -INFINITY;
    st.l = own ? 1.f : 0.f;
#pragma unroll
    for (int j = 0; j < 3; ++j) {
      const u32x4 z = own ? vtv[j] : u32x4{0u, 0u, 0u, 0u};
      st.a[2 * j] = f32x4{bflo(z[0]), bfhi(z[0]), bflo(z[1]), bfhi(z[1])};
      st.a[2 * j + 1] = f32x4{bflo(z[2]), bfhi(z[2]), bflo(z[3]), bfhi(z[3])};
    }
  }
  if constexpr (MODE >= 1) tag = __builtin_amdgcn_readfirstlane(tag);
  gu64* ticket = (gu64*)(wsh + 256);
  if constexpr (MODE == 2) {
    if (s == 0 && tid == 0) __hip_atomic_store(ticket, (unsigned long long)tag << 32, RLX_AGENT);
  }
  asm volatile("" : "+v"(st.m), "+v"(st.l), "+v"(st.a[0]), "+v"(st.a[1]), "+v"(st.a[2]), "+v"(st.a[3]), "+v"(st.a[4]), "+v"(st.a[5]) :: "memory");
  __builtin_amdgcn_sched_barrier(0);
  STAMP(1);

  if constexpr (MODE != 2) {
    for (int it = 0; it < NST - 1; ++it) {
      compute<T>(st, A, qv);
      load_tiles<T>(A, ks, vs, loff, so);
      so += STEPB;
      compute<T>(st, Bf, qv);
      load_tiles<T>(Bf, ks, vs, loff, so);
      so += STEPB;
    }
    compute<T>(st, A, qv);
    compute<T>(st, Bf, qv);
  } else {
    static_assert(MODE != 2 || NST >= 2, "");
    constexpr unsigned DYN0 = NST * NS * W * 2 * T;
    constexpr unsigned NTK = (NTILE - DYN0) / (2 * T);
    unsigned long long tk = 0;
    { unsigned zero; asm volatile("v_mov_b32 %0, 0" : "=v"(zero)); ticket = (gu64*)((GAS char*)ticket + zero); }
#pragma nounroll
    for (int it = 0;; ++it) {
      compute<T>(st, A, qv);
      unsigned soA = so, soB = so + STEPB;
      so += 2 * STEPB;
      if (it >= NST - 1) {
        unsigned hi = __builtin_amdgcn_readfirstlane((unsigned)(tk >> 32)), n = __builtin_amdgcn_readfirstlane((unsigned)tk);
#pragma nounroll
        for (unsigned spin = 0; hi != tag; ++spin) {
          if (spin > (1u << 20)) { n = 0xffffu; break; }
          __builtin_amdgcn_s_sleep(8);
          if (lane == 0) tk = __hip_atomic_fetch_add(ticket, 1ull, RLX_AGENT);
          hi = __builtin_amdgcn_readfirstlane((unsigned)(tk >> 32)); n = __builtin_amdgcn_readfirstlane((unsigned)tk);
        }
        if (n >= NTK) { nit_ = it; break; }
        soA = (DYN0 + n * 2 * T) * TILEB; soB = soA + T * TILEB;
      }
      load_tiles<T>(A, ks, vs, loff, soA);
      compute<T>(st, Bf, qv);
      if (it >= NST - 2 && lane == 0) tk = __hip_atomic_fetch_add(ticket, 1ull, RLX_AGENT);
      asm volatile("" ::: "memory");
      load_tiles<T>(Bf, ks, vs, loff, soB);
    }
    compute<T>(st, Bf, qv);
  }
  STAMP(2);
  const float M = xmax(st.m);
  const float w = ex2(st.m - M);
  const float l = xsum(st.l * w);
#pragma unroll
  for (int k = 0; k < 6; ++k)
#pragma unroll
    for (int e = 0; e < 4; ++e) st.a[k][e] = xsum(st.a[k][e] * w);
  float* rec = lds + wave * PREC;
  if (r == 0) {
#pragma unroll
    for (int j = 0; j < 3; ++j) {
      *reinterpret_cast<f32x4*>(rec + 4 + j * 64 + c * 8) = st.a[2 * j];
      *reinterpret_cast<f32x4*>(rec + 4 + j * 64 + c * 8 + 4) = st.a[2 * j + 1];
    }
    if (c == 0) { rec[0] = M; rec[1] = l; }
  }
  STAMP(3);
  __syncthreads();
  STAMP(4);
  if (W * 64 > D_ && tid >= D_) { DUMP(); return; }
  float MM = lds[0];
#pragma unroll
  for (int w2 = 1; w2 < W; ++w2) MM = fmaxf(MM, lds[w2 * PREC]);
  float L = 0.f, o = 0.f;
#pragma unroll
  for (int w2 = 0; w2 < W; ++w2) {
    const float wt = ex2(lds[w2 * PREC] - MM);
    L = fmaf(wt, lds[w2 * PREC + 1], L);
    o = fmaf(wt, lds[w2 * PREC + 4 + tid], o);
  }
  if constexpr (MODE == 0) {
    float* pr = (float*)(wsh + WS_SLOT0 + s * WS_SLOT);
    pr[4 + tid] = o;
    if (tid == 0) { pr[0] = MM; pr[1] = L; }
    STAMP(5); DUMP();
  } else {
    const unsigned long long th = (unsigned long long)tag << 32;
    if (s != 0) {
      gu64* g = (gu64*)(wsh + WS_SLOT0 + s * WS_SLOT);
      __hip_atomic_store(g + 2 + tid, th | __float_as_uint(o), RLX_AGENT);
      if (tid < 2) __hip_atomic_store(g + tid, th | __float_as_uint(tid == 0 ? MM : L), RLX_AGENT);
      STAMP(5); DUMP();
      return;
    }
    float pm[NS], pl[NS], po[NS];
    pm[0] = MM; pl[0] = L; po[0] = o;
#pragma nounroll
    for (unsigned spin = 0;; ++spin) {
      bool ok = true;
#pragma unroll
      for (int k = 1; k < NS; ++k) {
        gu64* g = (gu64*)(wsh + WS_SLOT0 + k * WS_SLOT);
        const unsigned long long x0 = __hip_atomic_load(g, RLX_AGENT), x1 = __hip_atomic_load(g + 1, RLX_AGENT), x2 = __hip_atomic_load(g + 2 + tid, RLX_AGENT);
        pm[k] = __uint_as_float((unsigned)x0); pl[k] = __uint_as_float((unsigned)x1); po[k] = __uint_as_float((unsigned)x2);
        ok &= ((unsigned)(x0 >> 32) == tag) & ((unsigned)(x1 >> 32) == tag) & ((unsigned)(x2 >> 32) == tag);
      }
      if (__all(ok)) break;
      if (spin > (1u << 20)) { pl[0] = __builtin_nanf(""); break; }
      __builtin_amdgcn_s_sleep(1);
    }
    float M2 = pm[0];
#pragma unroll
    for (int k = 1; k < NS; ++k) M2 = fmaxf(M2, pm[k]);
    float L2 = 0.f, o2 = 0.f;
#pragma unroll
    for (int k = 0; k < NS; ++k) { const float wt = ex2(pm[k] - M2); L2 = fmaf(wt, pl[k], L2); o2 = fmaf(wt, po[k], o2); }
    out[bh * D_ + tid] = __builtin_bit_cast(unsigned short, (__bf16)(o2 / L2));
    if (tid == 0) __hip_atomic_store((gu32*)wsh, tag, RLX_AGENT);
    STAMP(5); DUMP();
  }
}

template <int NS>
__global__ void __launch_bounds__(192) attn_combine(const unsigned char* __restrict__ ws, unsigned short* __restrict__ out) {
  const int bh = blockIdx.x, tid = threadIdx.x;
  float ms[NS], ls[NS], os[NS];
#pragma unroll
  for (int s = 0; s < NS; ++s) { const float* pr = (const float*)(ws + (size_t)bh * WS_HEAD + WS_SLOT0 + s * WS_SLOT); ms[s] = pr[0]; ls[s] = pr[1]; os[s] = pr[4 + tid]; }
  float M = ms[0];
#pragma unroll
  for (int s = 1; s < NS; ++s) M = fmaxf(M, ms[s]);
  float L = 0.f, o = 0.f;
#pragma unroll
  for (int s = 0; s < NS; ++s) { const float w = ex2(ms[s] - M); L = fmaf(w, ls[s], L); o = fmaf(w, os[s], o); }
  out[bh * D_ + tid] = __builtin_bit_cast(unsigned short, (__bf16)(o / L));
}
}

namespace tb {
using namespace ta;
typedef short bf16x8 __attribute__((ext_vector_type(8)));
typedef unsigned u32x2 __attribute__((ext_vector_type(2)));
constexpr int WS2_HEAD = 20736, WS2_SLOT0 = 256, WS2_SLOT = 1664;

template <int SCHED> __device__ __forceinline__ void unit_pos(unsigned u, unsigned& so, unsigned& drb) {
  if constexpr (SCHED == 0) { so = u * TILEB; drb = 4 * ROWB; }
  else if constexpr (SCHED == 1) {
    const unsigned x = u & 127u; const bool a = x >= 43u, b = x >= 85u;
    so = ((u >> 7) * 1024u + 4u * x + (a ? 172u : 0u) + (b ? 168u : 0u)) * ROWB; drb = ((a && !b) ? 168u : 172u) * ROWB;
  } else {
    const unsigned x = u & 63u; const bool a = x >= 21u, b = x >= 43u;
    so = ((u >> 6) * 512u + 4u * x + (a ? 84u : 0u) + (b ? 88u : 0u)) * ROWB; drb = ((a && !b) ? 88u : 84u) * ROWB;
  }
}

template <int NS, int W, int NB, int SCHED, int ROT, int DBG>
__global__ void __launch_bounds__(W * 64) attn2(const unsigned char* __restrict__ q, const unsigned char* __restrict__ kc,
                                                const unsigned char* __restrict__ vc, const unsigned char* __restrict__ kt,
                                                const unsigned char* __restrict__ vt, const unsigned short* __restrict__ tm,
                                                unsigned short* __restrict__ out, unsigned char* __restrict__ ws) {
  constexpr int NW = NS * W;
  constexpr int N = NTILE / NW;
  constexpr int SR = N + 4;
  constexpr int REC = 2 * D_ + 4;
  static_assert(N % 32 == 0 && N % NB == 0 && (NB == 2 || NB == 4), "");
  __shared__ __attribute__((aligned(16))) float lds[W * 8 * SR + W * REC];
  unsigned long long ts[6] = {0, 0, 0, 0, 0, 0};
  int nit_ = 0;
  STAMP(0);
  const int s = blockIdx.x % NS, bh = blockIdx.x / NS;
  const int tid = threadIdx.x, lane = tid & 63, wave = __builtin_amdgcn_readfirstlane(tid >> 6);
  const int r = lane >> 3, c = lane & 7;
  const unsigned lbase = (r & 3) * ROWB + c * 16, lhi = r >> 2;
  const unsigned char* kh = kc + (size_t)bh * HEADB;
  const unsigned char* vh = vc + (size_t)bh * HEADB;
  const unsigned u0 = s * W + wave + bh * ROT;
  u32x4 buf[NB][3];
  struct Addr { const unsigned char* base; unsigned nrec, so, vo; };
  auto prep = [&](int i) {
    const unsigned u = (u0 + (unsigned)i * NW) & (NTILE - 1);
    Addr a; unsigned drb; unit_pos<SCHED>(u, a.so, drb);
    a.base = i < N ? kh : vh; a.nrec = i < 2 * N ? HEADB : 0;
    a.vo = lbase + lhi * drb;
    asm volatile("" : "+s"(a.base), "+s"(a.nrec), "+s"(a.so), "+v"(a.vo));
    __builtin_amdgcn_sched_barrier(0);
    return a;
  };
  auto issue = [&](u32x4 (&dst)[3], const Addr& a) {
    const auto rs = __builtin_amdgcn_make_buffer_rsrc((void*)a.base, 0, a.nrec, 0x00020000);
#pragma unroll
    for (int j = 0; j < 3; ++j) dst[j] = __builtin_amdgcn_raw_buffer_load_b128(rs, a.vo + j * 128, a.so, 2);
    asm volatile("" ::: "memory");
    __builtin_amdgcn_sched_barrier(0);
  };
#pragma unroll
  for (int b = 0; b < NB; ++b) issue(buf[b], prep(b));
  unsigned char* wsh = ws + (size_t)bh * WS2_HEAD;
  u32x4 qv[3], ktv[3];
#pragma unroll
  for (int j = 0; j < 3; ++j) qv[j] = *reinterpret_cast<const u32x4*>(q + bh * ROWB + j * 128 + c * 16);
#pragma unroll
  for (int j = 0; j < 3; ++j) ktv[j] = *reinterpret_cast<const u32x4*>(kt + bh * ROWB + j * 128 + c * 16);
  u32x2 vtv = *reinterpret_cast<const u32x2*>(vt + bh * ROWB + (lane < 48 ? lane : 0) * 8);
  const unsigned mk = tm[bh / H_];
  unsigned tag = __hip_atomic_load((gu32*)wsh, RLX_AGENT) * 0x9E3779B1u + 0x7F4A7C15u;
  float sct;
  {
    float d0 = 0.f, d1 = 0.f, d2 = 0.f;
#pragma unroll
    for (int k = 0; k < 4; ++k) { d0 = dot2(ktv[0][k], qv[0][k], d0); d1 = dot2(ktv[1][k], qv[1][k], d1); d2 = dot2(ktv[2][k], qv[2][k], d2); }
    sct = sum8((d0 + d1) + d2) * SCALE_LOG2E + (1.0f - bflo(mk)) * (-1.0e9f * LOG2E);
  }
  tag = __builtin_amdgcn_readfirstlane(tag);
  asm volatile("" : "+v"(sct), "+v"(vtv) :: "memory");
  __builtin_amdgcn_sched_barrier(0);
  STAMP(1);
  float* sc = lds + wave * 8 * SR + r * SR;
  float mx = -INFINITY;
#pragma nounroll
  for (int k = 0; k < N; k += NB) {
    float d[NB];
#pragma unroll
    for (int b = 0; b < NB; ++b) {
      const Addr nx = prep(k + b + NB);
      float d0 = 0.f, d1 = 0.f, d2 = 0.f;
#pragma unroll
      for (int e = 0; e < 4; ++e) { d0 = dot2(buf[b][0][e], qv[0][e], d0); d1 = dot2(buf[b][1][e], qv[1][e], d1); d2 = dot2(buf[b][2][e], qv[2][e], d2); }
      asm volatile("" : "+v"(d0), "+v"(d1), "+v"(d2) :: "memory");
      __builtin_amdgcn_sched_barrier(0);
      issue(buf[b], nx);
      d[b] = sum8((d0 + d1) + d2);
      mx = fmaxf(mx, d[b]);
    }
    if (c == 0) {
      if constexpr (NB == 2) *reinterpret_cast<f32x2*>(sc + k) = f32x2{d[0], d[1]};
      else *reinterpret_cast<f32x4*>(sc + k) = f32x4{d[0], d[1], d[2], d[3]};
    }
  }
  const float Mw = xmax(mx) * SCALE_LOG2E;
  float lsum = 0.f;
  __builtin_amdgcn_fence(__ATOMIC_ACQ_REL, "wavefront");
#pragma unroll
  for (int e = 0; e < N / 8; e += 4) {
    float* pp = sc + c * (N / 8) + e;
    const f32x4 x = *reinterpret_cast<const f32x4*>(pp);
    u32x4 y;
#pragma unroll
    for (int i = 0; i < 4; ++i) {
      const float p = ex2(fmaf(x[i], SCALE_LOG2E, -Mw));
      y[i] = __builtin_bit_cast(unsigned, bf16x2{(__bf16)p, (__bf16)p});
      lsum += bfhi(y[i]);
    }
    *reinterpret_cast<u32x4*>(pp) = y;
  }
  __builtin_amdgcn_fence(__ATOMIC_ACQ_REL, "wavefront");
  lsum = xsum(sum8(lsum));
  u32x4 msk;
#pragma unroll
  for (int i = 0; i < 4; ++i) msk[i] = (c >> 1) == i ? ((c & 1) ? 0xffff0000u : 0x0000ffffu) : 0u;
  f32x4 acc[3] = {{0.f, 0.f, 0.f, 0.f}, {0.f, 0.f, 0.f, 0.f}, {0.f, 0.f, 0.f, 0.f}};
  const unsigned* pu = reinterpret_cast<const unsigned*>(sc);
  unsigned pn[NB];
  auto loadp = [&](int k) {
    if constexpr (NB == 2) { const u32x2 t = *reinterpret_cast<const u32x2*>(pu + k); pn[0] = t[0]; pn[1] = t[1]; }
    else { const u32x4 t = *reinterpret_cast<const u32x4*>(pu + k); pn[0] = t[0]; pn[1] = t[1]; pn[2] = t[2]; pn[3] = t[3]; }
  };
  loadp(0);
#pragma nounroll
  for (int k = 0; k < N; k += NB) {
    unsigned pc[NB];
#pragma unroll
    for (int b = 0; b < NB; ++b) pc[b] = pn[b];
    loadp(k + NB);
#pragma unroll
    for (int b = 0; b < NB; ++b) {
      const Addr nx = prep(N + k + b + NB);
      const u32x4 a = {pc[b] & msk[0], pc[b] & msk[1], pc[b] & msk[2], pc[b] & msk[3]};
#pragma unroll
      for (int j = 0; j < 3; ++j) acc[j] = __builtin_amdgcn_mfma_f32_16x16x32_bf16(__builtin_bit_cast(bf16x8, a), __builtin_bit_cast(bf16x8, buf[b][j]), acc[j], 0, 0, 0);
      asm volatile("" : "+a"(acc[0]), "+a"(acc[1]), "+a"(acc[2]) :: "memory");
      __builtin_amdgcn_sched_barrier(0);
      issue(buf[b], nx);
    }
  }
  STAMP(2);
  float* rec = lds + W * 8 * SR + wave * REC;
  {
    const int h = lane >> 4, n = lane & 15;
    if ((h >> 1) == (n >> 3)) {
#pragma unroll
      for (int j = 0; j < 3; ++j) *reinterpret_cast<f32x4*>(rec + 4 + (h >> 1) * D_ + j * 64 + (n & 7) * 8 + (h & 1) * 4) = acc[j];
    }
    if (lane == 0) *reinterpret_cast<f32x2*>(rec) = f32x2{Mw, lsum};
  }
  STAMP(3);
  __syncthreads();
  STAMP(4);
  if (wave != 0) { DUMP(); return; }
  const float* recs = lds + W * 8 * SR;
  const int l4 = (lane < 48 ? lane : 0) * 4;
  float MM = recs[0];
#pragma unroll
  for (int w2 = 1; w2 < W; ++w2) MM = fmaxf(MM, recs[w2 * REC]);
  float L = 0.f; f32x4 o = {0.f, 0.f, 0.f, 0.f};
#pragma unroll
  for (int w2 = 0; w2 < W; ++w2) {
    const float wt = ex2(recs[w2 * REC] - MM);
    L = fmaf(wt, recs[w2 * REC + 1], L);
    o += wt * (*reinterpret_cast<const f32x4*>(recs + w2 * REC + 4 + l4) + *reinterpret_cast<const f32x4*>(recs + w2 * REC + 4 + D_ + l4));
  }
  const auto wr = __builtin_amdgcn_make_buffer_rsrc((void*)wsh, 0, WS2_HEAD, 0x00020000);
  if (s != 0) {
    const unsigned so = WS2_SLOT0 + s * WS2_SLOT;
    const u32x4 g0 = lane < 48 ? u32x4{__float_as_uint(o[0]), tag, __float_as_uint(o[1]), tag} : u32x4{__float_as_uint(MM), tag, __float_as_uint(L), tag};
    if (lane <= 48) __builtin_amdgcn_raw_buffer_store_b128(g0, wr, lane * 16, so, 16);
    if (lane < 48) __builtin_amdgcn_raw_buffer_store_b128(u32x4{__float_as_uint(o[2]), tag, __float_as_uint(o[3]), tag}, wr, lane * 16 + 832, so, 16);
    STAMP(5); DUMP();
    return;
  }
  u32x4 g[NS][3];
#pragma nounroll
  for (unsigned spin = 0;; ++spin) {
    asm volatile("" ::: "memory");
    bool ok = true;
#pragma unroll
    for (int k = 1; k < NS; ++k) {
      g[k][0] = __builtin_amdgcn_raw_buffer_load_b128(wr, l4 * 4, WS2_SLOT0 + k * WS2_SLOT, 16);
      g[k][1] = __builtin_amdgcn_raw_buffer_load_b128(wr, l4 * 4 + 832, WS2_SLOT0 + k * WS2_SLOT, 16);
      g[k][2] = __builtin_amdgcn_raw_buffer_load_b128(wr, 768, WS2_SLOT0 + k * WS2_SLOT, 16);
    }
#pragma unroll
    for (int k = 1; k < NS; ++k)
#pragma unroll
      for (int j = 0; j < 3; ++j) ok &= (g[k][j][1] == tag) & (g[k][j][3] == tag);
    if (__all(ok)) break;
    if (spin > (1u << 20)) { L = __builtin_nanf(""); break; }
  }
  float M2 = fmaxf(MM, sct);
#pragma unroll
  for (int k = 1; k < NS; ++k) M2 = fmaxf(M2, __uint_as_float(g[k][2][0]));
  const float w0 = ex2(MM - M2), wt = ex2(sct - M2);
  float L2 = fmaf(w0, L, wt);
  f32x4 o2 = w0 * o + wt * f32x4{bflo(vtv[0]), bfhi(vtv[0]), bflo(vtv[1]), bfhi(vtv[1])};
#pragma unroll
  for (int k = 1; k < NS; ++k) {
    const float wk = ex2(__uint_as_float(g[k][2][0]) - M2);
    L2 = fmaf(wk, __uint_as_float(g[k][2][2]), L2);
    o2 += wk * f32x4{__uint_as_float(g[k][0][0]), __uint_as_float(g[k][0][2]), __uint_as_float(g[k][1][0]), __uint_as_float(g[k][1][2])};
  }
  const float inv = 1.0f / L2;
  o2 *= inv;
  if (lane < 48) {
    const u32x2 ob = {__builtin_bit_cast(unsigned, bf16x2{(__bf16)o2[0], (__bf16)o2[1]}), __builtin_bit_cast(unsigned, bf16x2{(__bf16)o2[2], (__bf16)o2[3]})};
    *reinterpret_cast<u32x2*>(out + bh * D_ + l4) = ob;
  }
  if (lane == 0) __hip_atomic_store((gu32*)wsh, tag, RLX_AGENT);
  STAMP(5); DUMP();
}
}


extern "C" void kernel_launch(void* const* d_in, const int* in_sizes, int n_in, void* d_out, int out_size, void* d_ws, size_t ws_size,
                              hipStream_t stream) {
  tb::attn2<4,2,4,1,0,0><<<256, 128, 0, stream>>>((const unsigned char*)d_in[0], (const unsigned char*)d_in[1], (const unsigned char*)d_in[2], (const unsigned char*)d_in[3],
      (const unsigned char*)d_in[4], (const unsigned short*)d_in[5], (unsigned short*)d_out, (unsigned char*)d_ws);
}
```

```cpp
#include <hip/hip_runtime.h>
#include <hip/hip_bf16.h>
#include <stdint.h>

namespace ta {
constexpr int B_ = 2, H_ = 32, C_ = 8192, D_ = 192;
constexpr int BH = B_ * H_;
constexpr int ROWB = D_ * 2;
constexpr int TILEB = 8 * ROWB;
constexpr int HEADB = C_ * ROWB;
constexpr int NTILE = C_ / 8;
constexpr float LOG2E = 1.4426950408889634f;
constexpr float SCALE_LOG2E = 0.07216878364870322f * LOG2E;
constexpr int PREC = 196;
constexpr int WS_HEAD = 20736;
constexpr int WS_SLOT0 = 1024, WS_SLOT = 1792;

#define GAS __attribute__((address_space(1)))
typedef unsigned u32x4 __attribute__((ext_vector_type(4)));
typedef __bf16 bf16x2 __attribute__((ext_vector_type(2)));
typedef float f32x4 __attribute__((ext_vector_type(4)));
typedef float f32x2 __attribute__((ext_vector_type(2)));
typedef GAS unsigned gu32;
typedef GAS unsigned long long gu64;
#define RLX_AGENT __ATOMIC_RELAXED, __HIP_MEMORY_SCOPE_AGENT

__device__ __forceinline__ float dot2(unsigned a, unsigned b, float c) {
  return __builtin_amdgcn_fdot2_f32_bf16(__builtin_bit_cast(bf16x2, a), __builtin_bit_cast(bf16x2, b), c, false);
}
template <int CTRL> __device__ __forceinline__ float dpp(float x) {
  return __builtin_bit_cast(float, __builtin_amdgcn_mov_dpp(__builtin_bit_cast(int, x), CTRL, 0xf, 0xf, true));
}
constexpr int XOR1 = 0xB1, XOR2 = 0x4E, XOR7 = 0x141, XOR8 = 0x128;
__device__ __forceinline__ float sum8(float x) {
  x += dpp<XOR1>(x); x += dpp<XOR2>(x); x += dpp<XOR7>(x); return x;
}
__device__ __forceinline__ float xsum(float x) {
  x += dpp<XOR8>(x);
  auto s = __builtin_amdgcn_permlane16_swap(__float_as_uint(x), __float_as_uint(x), false, false);
  x = __uint_as_float(s[0]) + __uint_as_float(s[1]);
  auto t = __builtin_amdgcn_permlane32_swap(__float_as_uint(x), __float_as_uint(x), false, false);
  return __uint_as_float(t[0]) + __uint_as_float(t[1]);
}
__device__ __forceinline__ float xmax(float x) {
  x = fmaxf(x, dpp<XOR8>(x));
  auto s = __builtin_amdgcn_permlane16_swap(__float_as_uint(x), __float_as_uint(x), false, false);
  x = fmaxf(__uint_as_float(s[0]), __uint_as_float(s[1]));
  auto t = __builtin_amdgcn_permlane32_swap(__float_as_uint(x), __float_as_uint(x), false, false);
  return fmaxf(__uint_as_float(t[0]), __uint_as_float(t[1]));
}
__device__ __forceinline__ float bflo(unsigned u) { return __uint_as_float(u << 16); }
__device__ __forceinline__ float bfhi(unsigned u) { return __uint_as_float(u & 0xffff0000u); }
__device__ __forceinline__ float ex2(float x) { return __builtin_amdgcn_exp2f(x); }

struct State { float m, l; f32x4 a[6]; };

template <int T> struct Buf { u32x4 k[T][3], v[T][3]; };

template <int T>
__device__ __forceinline__ void load_tiles(Buf<T>& b, __amdgpu_buffer_rsrc_t ks, __amdgpu_buffer_rsrc_t vs, unsigned loff, unsigned so) {
#pragma unroll
  for (int t = 0; t < T; ++t)
#pragma unroll
    for (int j = 0; j < 3; ++j) b.k[t][j] = __builtin_amdgcn_raw_buffer_load_b128(ks, loff + t * TILEB + j * 128, so, 2);
#pragma unroll
  for (int t = 0; t < T; ++t)
#pragma unroll
    for (int j = 0; j < 3; ++j) b.v[t][j] = __builtin_amdgcn_raw_buffer_load_b128(vs, loff + t * TILEB + j * 128, so, 2);
  asm volatile("" ::: "memory");
  __builtin_amdgcn_sched_barrier(0);
}

template <int T>
__device__ __forceinline__ void compute(State& st, const Buf<T>& b, const u32x4 (&qv)[3]) {
  float sc[T];
  float mn = st.m;
#pragma unroll
  for (int t = 0; t < T; ++t) {
    float d0 = 0.f, d1 = 0.f, d2 = 0.f;
#pragma unroll
    for (int k = 0; k < 4; ++k) { d0 = dot2(b.k[t][0][k], qv[0][k], d0); d1 = dot2(b.k[t][1][k], qv[1][k], d1); d2 = dot2(b.k[t][2][k], qv[2][k], d2); }
    sc[t] = sum8((d0 + d1) + d2) * SCALE_LOG2E;
    mn = fmaxf(mn, sc[t]);
  }
  const float alpha = ex2(st.m - mn);
  st.m = mn;
  float l = st.l * alpha;
#pragma unroll
  for (int k = 0; k < 6; ++k) st.a[k] *= alpha;
#pragma unroll
  for (int t = 0; t < T; ++t) {
    const float p = ex2(sc[t] - mn);
    l += p;
#pragma unroll
    for (int j = 0; j < 3; ++j) {
      const f32x4 lo = {bflo(b.v[t][j][0]), bfhi(b.v[t][j][0]), bflo(b.v[t][j][1]), bfhi(b.v[t][j][1])};
      const f32x4 hi = {bflo(b.v[t][j][2]), bfhi(b.v[t][j][2]), bflo(b.v[t][j][3]), bfhi(b.v[t][j][3])};
      st.a[2 * j] = __builtin_elementwise_fma(f32x4{p, p, p, p}, lo, st.a[2 * j]);
      st.a[2 * j + 1] = __builtin_elementwise_fma(f32x4{p, p, p, p}, hi, st.a[2 * j + 1]);
    }
  }
  st.l = l;
  asm volatile("" : "+v"(st.m), "+v"(st.l), "+v"(st.a[0]), "+v"(st.a[1]), "+v"(st.a[2]), "+v"(st.a[3]), "+v"(st.a[4]), "+v"(st.a[5]) :: "memory");
  __builtin_amdgcn_sched_barrier(0);
}

template <int NS, int W, int T, int MODE, int NSTAT, int ILV, int DBG>
__global__ void __launch_bounds__(W * 64) attn_main(const unsigned char* __restrict__ q, const unsigned char* __restrict__ kc,
                                                    const unsigned char* __restrict__ vc, const unsigned char* __restrict__ kt,
                                                    const unsigned char* __restrict__ vt, const unsigned short* __restrict__ tm,
                                                    unsigned short* __restrict__ out, unsigned char* __restrict__ ws) {
  __shared__ __attribute__((aligned(16))) float lds[W * PREC];
  unsigned long long ts[6] = {0, 0, 0, 0, 0, 0};
#define STAMP(i) do { if constexpr (DBG) ts[i] = __builtin_amdgcn_s_memrealtime(); } while (0)
#define DUMP() do { if constexpr (DBG) { if ((threadIdx.x & 63) == 0) { unsigned long long* sp = (unsigned long long*)(ws + (64 << 20)) + (size_t)(blockIdx.x * W + (threadIdx.x >> 6)) * 8; \
    for (int i_ = 0; i_ < 6; ++i_) sp[i_] = ts[i_]; sp[6] = (unsigned)__builtin_amdgcn_s_getreg(20 | (3 << 11)) & 0xfu; sp[7] = nit_ | ((unsigned long long)((unsigned)__builtin_amdgcn_s_getreg(63492) >> 8 & 0xffu) << 32); } } } while (0)
  int nit_ = 0;
  STAMP(0);
  const int s = blockIdx.x % NS, bh = blockIdx.x / NS;
  const int tid = threadIdx.x, lane = tid & 63, wave = __builtin_amdgcn_readfirstlane(tid >> 6);
  const int r = lane >> 3, c = lane & 7;
  const unsigned loff = r * ROWB + c * 16;
  const auto ks = __builtin_amdgcn_make_buffer_rsrc((void*)(kc + (size_t)bh * HEADB), 0, HEADB, 0x00020000);
  const auto vs = __builtin_amdgcn_make_buffer_rsrc((void*)(vc + (size_t)bh * HEADB), 0, HEADB, 0x00020000);
  unsigned char* wsh = ws + (size_t)bh * WS_HEAD;
  constexpr int NSTEP_ALL = NTILE / (NS * W * 2 * T);
  constexpr int NST = (MODE == 2) ? NSTAT : NSTEP_ALL;
  constexpr unsigned STEPB = (ILV == 0) ? W * T * TILEB : NS * W * T * TILEB;
  unsigned so = (ILV == 0) ? (unsigned)(s * (NST * 2 * W * T) + wave * T) * TILEB : (unsigned)(s * W + wave) * T * TILEB;
  Buf<T> A, Bf;
  load_tiles<T>(A, ks, vs, loff, so);
  so += STEPB;
  u32x4 qv[3], ktv[3], vtv[3];
#pragma unroll
  for (int j = 0; j < 3; ++j) qv[j] = *reinterpret_cast<const u32x4*>(q + bh * ROWB + j * 128 + c * 16);
#pragma unroll
  for (int j = 0; j < 3; ++j) ktv[j] = *reinterpret_cast<const u32x4*>(kt + bh * ROWB + j * 128 + c * 16);
#pragma unroll
  for (int j = 0; j < 3; ++j) vtv[j] = *reinterpret_cast<const u32x4*>(vt + bh * ROWB + j * 128 + c * 16);
  const unsigned mk = tm[bh / H_];
  unsigned tag = 0;
  if constexpr (MODE >= 1) tag = __hip_atomic_load((gu32*)wsh, RLX_AGENT) * 0x9E3779B1u + 0x7F4A7C15u;
  asm volatile("" ::: "memory");
  __builtin_amdgcn_sched_barrier(0);
  load_tiles<T>(Bf, ks, vs, loff, so);
  so += STEPB;
  State st;
  {
    float d0 = 0.f, d1 = 0.f, d2 = 0.f;
#pragma unroll
    for (int k = 0; k < 4; ++k) { d0 = dot2(ktv[0][k], qv[0][k], d0); d1 = dot2(ktv[1][k], qv[1][k], d1); d2 = dot2(ktv[2][k], qv[2][k], d2); }
    const float sct = sum8((d0 + d1) + d2) * SCALE_LOG2E + (1.0f - bflo(mk)) * (-1.0e9f * LOG2E);
    const bool own = (s == 0) && (tid < 8);
    st.m = own ? sct : -INFINITY;
    st.l = own ? 1.f : 0.f;
#pragma unroll
    for (int j = 0; j < 3; ++j) {
      const u32x4 z = own ? vtv[j] : u32x4{0u, 0u, 0u, 0u};
      st.a[2 * j] = f32x4{bflo(z[0]), bfhi(z[0]), bflo(z[1]), bfhi(z[1])};
      st.a[2 * j + 1] = f32x4{bflo(z[2]), bfhi(z[2]), bflo(z[3]), bfhi(z[3])};
    }
  }
  if constexpr (MODE >= 1) tag = __builtin_amdgcn_readfirstlane(tag);
  gu64* ticket = (gu64*)(wsh + 256);
  if constexpr (MODE == 2) {
    if (s == 0 && tid == 0) __hip_atomic_store(ticket, (unsigned long long)tag << 32, RLX_AGENT);
  }
  asm volatile("" : "+v"(st.m), "+v"(st.l), "+v"(st.a[0]), "+v"(st.a[1]), "+v"(st.a[2]), "+v"(st.a[3]), "+v"(st.a[4]), "+v"(st.a[5]) :: "memory");
  __builtin_amdgcn_sched_barrier(0);
  STAMP(1);

  if constexpr (MODE != 2) {
    for (int it = 0; it < NST - 1; ++it) {
      compute<T>(st, A, qv);
      load_tiles<T>(A, ks, vs, loff, so);
      so += STEPB;
      compute<T>(st, Bf, qv);
      load_tiles<T>(Bf, ks, vs, loff, so);
      so += STEPB;
    }
    compute<T>(st, A, qv);
    compute<T>(st, Bf, qv);
  } else {
    static_assert(MODE != 2 || NST >= 2, "");
    constexpr unsigned DYN0 = NST * NS * W * 2 * T;
    constexpr unsigned NTK = (NTILE - DYN0) / (2 * T);
    unsigned long long tk = 0;
    { unsigned zero; asm volatile("v_mov_b32 %0, 0" : "=v"(zero)); ticket = (gu64*)((GAS char*)ticket + zero); }
#pragma nounroll
    for (int it = 0;; ++it) {
      compute<T>(st, A, qv);
      unsigned soA = so, soB = so + STEPB;
      so += 2 * STEPB;
      if (it >= NST - 1) {
        unsigned hi = __builtin_amdgcn_readfirstlane((unsigned)(tk >> 32)), n = __builtin_amdgcn_readfirstlane((unsigned)tk);
#pragma nounroll
        for (unsigned spin = 0; hi != tag; ++spin) {
          if (spin > (1u << 20)) { n = 0xffffu; break; }
          __builtin_amdgcn_s_sleep(8);
          if (lane == 0) tk = __hip_atomic_fetch_add(ticket, 1ull, RLX_AGENT);
          hi = __builtin_amdgcn_readfirstlane((unsigned)(tk >> 32)); n = __builtin_amdgcn_readfirstlane((unsigned)tk);
        }
        if (n >= NTK) { nit_ = it; break; }
        soA = (DYN0 + n * 2 * T) * TILEB; soB = soA + T * TILEB;
      }
      load_tiles<T>(A, ks, vs, loff, soA);
      compute<T>(st, Bf, qv);
      if (it >= NST - 2 && lane == 0) tk = __hip_atomic_fetch_add(ticket, 1ull, RLX_AGENT);
      asm volatile("" ::: "memory");
      load_tiles<T>(Bf, ks, vs, loff, soB);
    }
    compute<T>(st, Bf, qv);
  }
  STAMP(2);
  const float M = xmax(st.m);
  const float w = ex2(st.m - M);
  const float l = xsum(st.l * w);
#pragma unroll
  for (int k = 0; k < 6; ++k)
#pragma unroll
    for (int e = 0; e < 4; ++e) st.a[k][e] = xsum(st.a[k][e] * w);
  float* rec = lds + wave * PREC;
  if (r == 0) {
#pragma unroll
    for (int j = 0; j < 3; ++j) {
      *reinterpret_cast<f32x4*>(rec + 4 + j * 64 + c * 8) = st.a[2 * j];
      *reinterpret_cast<f32x4*>(rec + 4 + j * 64 + c * 8 + 4) = st.a[2 * j + 1];
    }
    if (c == 0) { rec[0] = M; rec[1] = l; }
  }
  STAMP(3);
  __syncthreads();
  STAMP(4);
  if (W * 64 > D_ && tid >= D_) { DUMP(); return; }
  float MM = lds[0];
#pragma unroll
  for (int w2 = 1; w2 < W; ++w2) MM = fmaxf(MM, lds[w2 * PREC]);
  float L = 0.f, o = 0.f;
#pragma unroll
  for (int w2 = 0; w2 < W; ++w2) {
    const float wt = ex2(lds[w2 * PREC] - MM);
    L = fmaf(wt, lds[w2 * PREC + 1], L);
    o = fmaf(wt, lds[w2 * PREC + 4 + tid], o);
  }
  if constexpr (MODE == 0) {
    float* pr = (float*)(wsh + WS_SLOT0 + s * WS_SLOT);
    pr[4 + tid] = o;
    if (tid == 0) { pr[0] = MM; pr[1] = L; }
    STAMP(5); DUMP();
  } else {
    const unsigned long long th = (unsigned long long)tag << 32;
    if (s != 0) {
      gu64* g = (gu64*)(wsh + WS_SLOT0 + s * WS_SLOT);
      __hip_atomic_store(g + 2 + tid, th | __float_as_uint(o), RLX_AGENT);
      if (tid < 2) __hip_atomic_store(g + tid, th | __float_as_uint(tid == 0 ? MM : L), RLX_AGENT);
      STAMP(5); DUMP();
      return;
    }
    float pm[NS], pl[NS], po[NS];
    pm[0] = MM; pl[0] = L; po[0] = o;
#pragma nounroll
    for (unsigned spin = 0;; ++spin) {
      bool ok = true;
#pragma unroll
      for (int k = 1; k < NS; ++k) {
        gu64* g = (gu64*)(wsh + WS_SLOT0 + k * WS_SLOT);
        const unsigned long long x0 = __hip_atomic_load(g, RLX_AGENT), x1 = __hip_atomic_load(g + 1, RLX_AGENT), x2 = __hip_atomic_load(g + 2 + tid, RLX_AGENT);
        pm[k] = __uint_as_float((unsigned)x0); pl[k] = __uint_as_float((unsigned)x1); po[k] = __uint_as_float((unsigned)x2);
        ok &= ((unsigned)(x0 >> 32) == tag) & ((unsigned)(x1 >> 32) == tag) & ((unsigned)(x2 >> 32) == tag);
      }
      if (__all(ok)) break;
      if (spin > (1u << 20)) { pl[0] = __builtin_nanf(""); break; }
      __builtin_amdgcn_s_sleep(1);
    }
    float M2 = pm[0];
#pragma unroll
    for (int k = 1; k < NS; ++k) M2 = fmaxf(M2, pm[k]);
    float L2 = 0.f, o2 = 0.f;
#pragma unroll
    for (int k = 0; k < NS; ++k) { const float wt = ex2(pm[k] - M2); L2 = fmaf(wt, pl[k], L2); o2 = fmaf(wt, po[k], o2); }
    out[bh * D_ + tid] = __builtin_bit_cast(unsigned short, (__bf16)(o2 / L2));
    if (tid == 0) __hip_atomic_store((gu32*)wsh, tag, RLX_AGENT);
    STAMP(5); DUMP();
  }
}

template <int NS>
__global__ void __launch_bounds__(192) attn_combine(const unsigned char* __restrict__ ws, unsigned short* __restrict__ out) {
  const int bh = blockIdx.x, tid = threadIdx.x;
  float ms[NS], ls[NS], os[NS];
#pragma unroll
  for (int s = 0; s < NS; ++s) { const float* pr = (const float*)(ws + (size_t)bh * WS_HEAD + WS_SLOT0 + s * WS_SLOT); ms[s] = pr[0]; ls[s] = pr[1]; os[s] = pr[4 + tid]; }
  float M = ms[0];
#pragma unroll
  for (int s = 1; s < NS; ++s) M = fmaxf(M, ms[s]);
  float L = 0.f, o = 0.f;
#pragma unroll
  for (int s = 0; s < NS; ++s) { const float w = ex2(ms[s] - M); L = fmaf(w, ls[s], L); o = fmaf(w, os[s], o); }
  out[bh * D_ + tid] = __builtin_bit_cast(unsigned short, (__bf16)(o / L));
}
}

#define LAS __attribute__((address_space(3)))
namespace th {
using namespace ta;
typedef short bf16x8 __attribute__((ext_vector_type(8)));
typedef unsigned u32x2 __attribute__((ext_vector_type(2)));
typedef const __attribute__((address_space(4))) unsigned cu32;
constexpr int WSH = 20736, SLOT0 = 256, SLOT = 1664;
constexpr int NB = 8, W = 2, SR = 88 + 12, REC = 2 * D_ + 4;
constexpr int L_SC = 0, L_REC = W * 8 * SR, L_CTL = L_REC + 2 * W * REC, L_END = L_CTL + 16;
constexpr unsigned OOB = 0x40000000u;
template <int V> struct IC { static constexpr int value = V; };

__device__ __forceinline__ unsigned unit_entry(unsigned h, unsigned cls, unsigned n) {
  const unsigned P = n >> 5, y = n & 31u, S = 48u * h + 3u * P;
  const unsigned f0 = (cls ^ (S >> 3) ^ (S >> 10)) & 1u, f1 = (cls ^ ((S + 1) >> 3) ^ ((S + 1) >> 10)) & 1u, f2 = (cls ^ ((S + 2) >> 3) ^ ((S + 2) >> 10)) & 1u;
  unsigned o0 = f0 * 11u, o1 = 31u + f0 + f1 * (11u - f0 + f2), o2 = 64u + f2 * 11u;
  asm volatile("" : "+v"(o0), "+v"(o1), "+v"(o2));
  const bool a = y < 11u - f0, b = y < 22u - f2;
  return (128u * P + y + (a ? o0 : (b ? o1 : o2))) * (4u * ROWB) + ((!a && b) ? 1u : 0u);
}

template <int THL, int TH, int THH, int DELTA, int TAU, int NPUB, int PER, int SLP, int PM, int DBG>
__global__ void __launch_bounds__(192) attn9(const unsigned char* __restrict__ q, const unsigned char* __restrict__ kc,
                                             const unsigned char* __restrict__ vc, const unsigned char* __restrict__ kt,
                                             const unsigned char* __restrict__ vt, const unsigned short* __restrict__ tm,
                                             unsigned short* __restrict__ out, unsigned char* __restrict__ ws) {
  __shared__ __attribute__((aligned(16))) float lds[L_END];
  unsigned long long ts[6] = {0, 0, 0, 0, 0, 0};
  int nit_ = 0;
  STAMP(0);
  __builtin_amdgcn_sched_barrier(0);
  const unsigned blk = blockIdx.x;
  const int tid = threadIdx.x, lane = tid & 63, wave = __builtin_amdgcn_readfirstlane(tid >> 6);
  const int r = lane >> 3, c = lane & 7;
  const unsigned cls = (blk >> 2) & 1u, ci = (blk >> 3) * 4u + (blk & 3u);
  const unsigned h1 = (ci * 171u) >> 9, mA = ci - 3u * h1;
  const unsigned hq = ((ci + 4u) * 171u) >> 10, h0 = 42u + hq, mB = ci + 4u - 6u * hq;
  const unsigned n0 = 40u + (mA & 1u) * 8u;
  const unsigned st0 = mB * 80u + ((mB + 1u) >> 2) * 16u + wave, st1 = mA * 176u - (mA >> 1) * 16u + wave - 2u * n0;
  unsigned te0;
  { const bool g = (unsigned)lane >= n0; te0 = unit_entry(g ? h1 : h0, cls, (g ? st1 : st0) + 2u * lane); }
  const unsigned vo84 = (r & 3) * ROWB + c * 16 + (r >> 2) * (84 * ROWB), vstep = (r >> 2) * (4 * ROWB);
  volatile LAS unsigned* ctl = (volatile LAS unsigned*)(lds + L_CTL);
  const unsigned slot1 = cls * (h1 == 42u ? 2u : 3u) + mA, slot0 = cls * (h0 == 42u ? 2u : 6u) + mB;
  const int l4 = (lane < 48 ? lane : 0) * 4;
  auto merge_publish = [&](int g, unsigned tag, bool with_ml) {
    const float* recs = lds + L_REC + g * W * REC;
    const f32x2 ml0 = *reinterpret_cast<const f32x2*>(recs), ml1 = *reinterpret_cast<const f32x2*>(recs + REC);
    const f32x4 a0 = *reinterpret_cast<const f32x4*>(recs + 4 + l4), a1 = *reinterpret_cast<const f32x4*>(recs + 4 + D_ + l4);
    const f32x4 b0 = *reinterpret_cast<const f32x4*>(recs + REC + 4 + l4), b1 = *reinterpret_cast<const f32x4*>(recs + REC + 4 + D_ + l4);
    const float MM = fmaxf(ml0[0], ml1[0]), w0 = ex2(ml0[0] - MM), w1 = ex2(ml1[0] - MM);
    const f32x4 o = w0 * (a0 + a1) + w1 * (b0 + b1);
    const auto wr = __builtin_amdgcn_make_buffer_rsrc((void*)(ws + (size_t)(g ? h1 : h0) * WSH), 0, WSH, 0x00020000);
    const unsigned so = SLOT0 + (g ? slot1 : slot0) * SLOT;
    const u32x4 g0 = lane < 48 ? u32x4{__float_as_uint(o[0]), tag, __float_as_uint(o[1]), tag} : u32x4{__float_as_uint(MM), tag, __float_as_uint(fmaf(w0, ml0[1], w1 * ml1[1])), tag};
    __builtin_amdgcn_raw_buffer_store_b128(g0, wr, (with_ml ? lane <= 48 : lane < 48) ? lane * 16 : OOB, so, 16);
    __builtin_amdgcn_raw_buffer_store_b128(u32x4{__float_as_uint(o[2]), tag, __float_as_uint(o[3]), tag}, wr, lane < 48 ? lane * 16 + 896 : OOB, so, 16);
  };
  asm volatile("" :: "s"(q), "s"(kc), "s"(vc), "v"(te0));

  if (__builtin_expect(wave < W, 1)) {
    unsigned te1 = 0;
    u32x4 buf[NB][3];
    struct Addr { unsigned so, vo; };
    auto prep = [&](unsigned tsel, int e) {
      const unsigned x = __builtin_amdgcn_readlane(tsel, e);
      Addr a; a.so = x & ~1u; a.vo = vo84 + vstep * (x & 1u);
      asm volatile("" : "+s"(a.so));
      asm volatile("" : "+v"(a.vo));
      __builtin_amdgcn_sched_barrier(0);
      return a;
    };
    auto issue = [&](u32x4 (&dst)[3], __amdgpu_buffer_rsrc_t rs, const Addr& a, auto mode, int on) {
      if constexpr (mode.value == 1) asm volatile("s_waitcnt vmcnt(%0)" :: "i"(TH) : "memory");
      if constexpr (mode.value == 3) asm volatile("s_waitcnt vmcnt(%0)" :: "i"(TH + 6) : "memory");
      if constexpr (mode.value == 2) asm volatile("s_cmp_lt_i32 %3, 0\n\ts_waitcnt vmcnt(%0)\n\ts_cbranch_scc1 1f\n\ts_cmp_le_i32 %3, %4\n\ts_waitcnt vmcnt(%1)\n\ts_cbranch_scc1 1f\n\ts_waitcnt vmcnt(%2)\n1:"
                                                  :: "i"(THH), "i"(TH), "i"(THL), "s"(on), "i"(2 * DELTA) : "scc", "memory");
#pragma unroll
      for (int j = 0; j < 3; ++j) dst[j] = __builtin_amdgcn_raw_buffer_load_b128(rs, a.vo + j * 128, a.so, 2);
      asm volatile("" ::: "memory");
      __builtin_amdgcn_sched_barrier(0);
    };
    auto rsrc = [&](const unsigned char* p, unsigned h, unsigned bytes) { return __builtin_amdgcn_make_buffer_rsrc((void*)(p + (size_t)h * HEADB), 0, bytes, 0x00020000); };
    constexpr int NPRE = (TH + 3) / 3;
    u32x4 qv[3], qn[3];
    {
      const auto rs = rsrc(kc, h0, HEADB);
      if (DBG) STAMP(4);
#pragma unroll
      for (int b = 0; b < NPRE; ++b) issue(buf[b], rs, prep(te0, b), IC<0>{}, 0);
#pragma unroll
      for (int j = 0; j < 3; ++j) qv[j] = *reinterpret_cast<const u32x4*>(q + h0 * ROWB + j * 128 + c * 16);
#pragma unroll
      for (int j = 0; j < 3; ++j) qn[j] = *reinterpret_cast<const u32x4*>(q + h1 * ROWB + j * 128 + c * 16);
      asm volatile("" ::: "memory");
      __builtin_amdgcn_sched_barrier(0);
      if (DBG) STAMP(5);
      te1 = unit_entry(h1, cls, st1 + 128u + 2u * lane);
      asm volatile("" : "+v"(te1));
      __builtin_amdgcn_s_barrier();
      __builtin_amdgcn_sched_barrier(0);
#pragma unroll
      for (int b = NPRE; b < NB; ++b) { if (b < 2 * NPRE) issue(buf[b], rs, prep(te0, b), IC<3>{}, 0); else issue(buf[b], rs, prep(te0, b), IC<1>{}, 0); }
    }
    STAMP(1);
    int err = DELTA;
    unsigned rt = 0, re = 0, tagw = 0;
    unsigned prog = 0;
    const unsigned zoff = h1 == 42u ? 60u : 0u;
    auto pace_step = [&]() {
      const unsigned zr = ctl[2], zt = ctl[3]; const float sl = __uint_as_float(ctl[7]);
      const unsigned now = (unsigned)__builtin_amdgcn_s_memrealtime();
      const unsigned z = now - prog + zoff;
      if (tid == 0) { ctl[0] = z; ctl[1] = now; if (prog == 29u * TAU) ctl[4] = 1u; }
      if constexpr (DBG) { rt = (unsigned)lane == prog / TAU ? now : rt; re = (unsigned)lane == prog / TAU ? (unsigned)err : re; }
      prog += TAU;
      int ei = (now - zt) < 1500u ? (int)(zr - z) + (int)(sl * (float)(now - zt)) + DELTA : DELTA;
      asm volatile("" : "+v"(ei));
      err = __builtin_amdgcn_readfirstlane(ei);
      asm volatile("" ::: "memory");
      __builtin_amdgcn_sched_barrier(0);
    };
    float* sc = lds + L_SC + wave * 8 * SR + r * SR;
    const unsigned* pu = reinterpret_cast<const unsigned*>(sc);
    u32x4 msk;
#pragma unroll
    for (int i = 0; i < 4; ++i) msk[i] = (c >> 1) == i ? ((c & 1) ? 0xffff0000u : 0x0000ffffu) : 0u;
#pragma nounroll
    for (int g = 0; g < 2; ++g) {
      const int n = g ? 128 - n0 : n0, e0 = g ? n0 : 0;
      const unsigned h = g ? h1 : h0;
      float mx = -INFINITY;
#pragma nounroll
      for (int k = 0; k < n; k += NB) {
        pace_step();
        const bool last = k + NB == n;
        const auto rs = rsrc(last ? vc : kc, h, HEADB);
        const int en = last ? e0 : e0 + k + NB;
        const unsigned tsel = (en & 64) ? te1 : te0;
        float d[NB];
#pragma unroll
        for (int b = 0; b < NB; ++b) {
          const Addr nx = prep(tsel, (en & 63) + b);
          float d0 = 0.f, d1 = 0.f, d2 = 0.f;
#pragma unroll
          for (int e = 0; e < 4; ++e) { d0 = dot2(buf[b][0][e], qv[0][e], d0); d1 = dot2(buf[b][1][e], qv[1][e], d1); d2 = dot2(buf[b][2][e], qv[2][e], d2); }
          asm volatile("" : "+v"(d0), "+v"(d1), "+v"(d2) :: "memory");
          __builtin_amdgcn_sched_barrier(0);
          issue(buf[b], rs, nx, IC<2>{}, err);
          d[b] = sum8((d0 + d1) + d2);
          mx = fmaxf(mx, d[b]);
          if ((b & 3) == 3 && c == 0) *reinterpret_cast<f32x4*>(sc + k + b - 3) = f32x4{d[b - 3], d[b - 2], d[b - 1], d[b]};
        }
      }
      const float Mw = xmax(mx) * SCALE_LOG2E;
      float lsum = 0.f;
      __builtin_amdgcn_fence(__ATOMIC_ACQ_REL, "wavefront");
      {
        f32x4 x[3];
#pragma unroll
        for (int e = 0; e < 3; ++e) x[e] = *reinterpret_cast<const f32x4*>(sc + e * 32 + c * 4);
#pragma unroll
        for (int e = 0; e < 3; ++e) {
          const bool in = e * 32 + c * 4 < n;
          u32x4 y;
#pragma unroll
          for (int i = 0; i < 4; ++i) {
            const float p = in ? ex2(fmaf(x[e][i], SCALE_LOG2E, -Mw)) : 0.f;
            y[i] = __builtin_bit_cast(unsigned, bf16x2{(__bf16)p, (__bf16)p});
            lsum += bfhi(y[i]);
          }
          *reinterpret_cast<u32x4*>(sc + e * 32 + c * 4) = y;
        }
      }
      __builtin_amdgcn_fence(__ATOMIC_ACQ_REL, "wavefront");
      lsum = xsum(sum8(lsum));
      float* rec = lds + L_REC + (g * W + wave) * REC;
      if (lane == 0) *reinterpret_cast<f32x2*>(rec) = f32x2{Mw, lsum};
      f32x4 acc[3] = {{0.f, 0.f, 0.f, 0.f}, {0.f, 0.f, 0.f, 0.f}, {0.f, 0.f, 0.f, 0.f}};
#pragma nounroll
      for (int k = 0; k < n; k += NB) {
        const u32x4 pa = *reinterpret_cast<const u32x4*>(pu + k), pb = *reinterpret_cast<const u32x4*>(pu + k + 4);
        pace_step();
        const bool last = k + NB == n;
        if (last && g) {
          if (lane == 0) ctl[5 + wave] = 1u;
          for (unsigned spin = 0; ctl[9] == 0u && spin < (1u << 24); ++spin) {}
          tagw = ctl[8];
        }
        const auto rs = rsrc(last ? kc : vc, last ? h1 : h, (last && g) ? 0u : (unsigned)HEADB);
        const int en = e0 + k + NB;
        const unsigned tsel = (en & 64) ? te1 : te0;
#pragma unroll
        for (int b = 0; b < NB; ++b) {
          const Addr nx = prep(tsel, (en & 63) + b);
          const unsigned p = b < 4 ? pa[b & 3] : pb[b & 3];
          const u32x4 a = {p & msk[0], p & msk[1], p & msk[2], p & msk[3]};
#pragma unroll
          for (int j = 0; j < 3; ++j) acc[j] = __builtin_amdgcn_mfma_f32_16x16x32_bf16(__builtin_bit_cast(bf16x8, a), __builtin_bit_cast(bf16x8, buf[b][j]), acc[j], 0, 0, 0);
          asm volatile("" : "+a"(acc[0]), "+a"(acc[1]), "+a"(acc[2]) :: "memory");
          __builtin_amdgcn_sched_barrier(0);
          issue(buf[b], rs, nx, IC<2>{}, err);
        }
      }
      if (g) STAMP(2);
      {
        const int hh = lane >> 4, nn = lane & 15;
        if ((hh >> 1) == (nn >> 3)) {
#pragma unroll
          for (int j = 0; j < 3; ++j) *reinterpret_cast<f32x4*>(rec + 4 + (hh >> 1) * D_ + j * 64 + (nn & 7) * 8 + (hh & 1) * 4) = acc[j];
        }
      }
#pragma unroll
      for (int j = 0; j < 3; ++j) qv[j] = qn[j];
    }
    {
      asm volatile("" ::: "memory");
      unsigned old = 0;
      if (lane == 0) old = __hip_atomic_fetch_add((LAS unsigned*)(lds + L_CTL + 10), 1u, __ATOMIC_RELAXED, __HIP_MEMORY_SCOPE_WORKGROUP);
      old = __builtin_amdgcn_readfirstlane(old);
      asm volatile("" ::: "memory");
      if (old == 1u) merge_publish(1, tagw, false);
    }
    if constexpr (DBG) { unsigned* rp = (unsigned*)(ws + (72 << 20)) + (size_t)(blk * W + wave) * 128; rp[lane] = rt; rp[64 + lane] = re; }
    if constexpr (DBG) { STAMP(3); if (lane == 0) { unsigned long long* sp = (unsigned long long*)(ws + (64 << 20)) + (size_t)(blk * W + wave) * 8;
      for (int i_ = 0; i_ < 4; ++i_) sp[i_] = ts[i_]; if (PM & 64) { sp[2] = ts[4]; sp[3] = ts[5]; } sp[6] = (unsigned)__builtin_amdgcn_s_getreg(20 | (3 << 11)) & 0xfu; sp[7] = 0; } }
    return;
  }

  if (lane < 16) ctl[lane] = 0u;
  asm volatile("s_waitcnt lgkmcnt(0)" ::: "memory");
  __builtin_amdgcn_s_barrier();
  constexpr unsigned PSTR = 256 / NPUB + 1;
  const bool pub = (blk % PSTR) == 0u && (blk / PSTR) < (unsigned)NPUB;
  GAS unsigned char* pace = (GAS unsigned char*)(ws + (size_t)BH * WSH);
  const unsigned nsl0 = h0 == 42u ? 8u : 12u, nsl1 = h1 == 42u ? 8u : 6u;
  const unsigned dim0 = 16u * slot0 + (h0 == 42u ? 64u : 0u) + 4u * (lane & 3) + 2u * ((lane >> 2) & 1);
  const unsigned dim1 = 32u * slot1 + 4u * (lane & 7) + 2u * ((lane >> 3) & 1);
  unsigned tag0, tag1;
  float sct0, sct1; unsigned vt0, vt1;
  {
    const unsigned e0 = *(cu32*)(ws + (size_t)h0 * WSH), e1 = *(cu32*)(ws + (size_t)h1 * WSH), mk2 = *(cu32*)tm;
    if (PM & 1) {
      unsigned w0 = *(cu32*)(kt + h0 * ROWB), w1 = *(cu32*)(vt + h0 * ROWB), w2 = *(cu32*)(out + h0 * D_);
      asm volatile("" :: "s"(w0), "s"(w1), "s"(w2) : "memory");
      __builtin_amdgcn_s_sleep(127);
      asm volatile("" ::: "memory");
    }
    const unsigned hh = lane < 32 ? h0 : h1;
    float d0 = 0.f;
#pragma unroll
    for (int j = 0; j < 3; ++j) {
      const u32x4 a = *reinterpret_cast<const u32x4*>(q + hh * ROWB + j * 128 + c * 16), b = *reinterpret_cast<const u32x4*>(kt + hh * ROWB + j * 128 + c * 16);
#pragma unroll
      for (int k = 0; k < 4; ++k) d0 = dot2(a[k], b[k], d0);
    }
    vt0 = *reinterpret_cast<const unsigned*>(vt + h0 * ROWB + dim0 * 2);
    vt1 = *reinterpret_cast<const unsigned*>(vt + h1 * ROWB + dim1 * 2);
    const float sc2 = sum8(d0) * SCALE_LOG2E + (1.0f - (hh >= H_ ? bfhi(mk2) : bflo(mk2))) * (-1.0e9f * LOG2E);
    sct0 = __uint_as_float(__builtin_amdgcn_readlane(__float_as_uint(sc2), 0));
    sct1 = __uint_as_float(__builtin_amdgcn_readlane(__float_as_uint(sc2), 32));
    tag0 = e0 * 0x9E3779B1u + 0x7F4A7C15u;
    tag1 = e1 * 0x9E3779B1u + 0x7F4A7C15u;
    if (lane == 0) { ctl[8] = tag1; ctl[9] = 1u; }
  }
  asm volatile("" : "+v"(vt0), "+v"(vt1) :: "memory");
  {
    unsigned kslot = (unsigned)__builtin_amdgcn_s_memrealtime() / PER + 1, half = 0, pzr = 0, pzt = 0;
#pragma nounroll
    for (unsigned spin = 0; spin < (1u << 24); ++spin) {
      if (ctl[4] != 0u) break;
      const unsigned now = (unsigned)__builtin_amdgcn_s_memrealtime();
      if ((int)(now - (kslot * PER + half * (PER / 2))) >= 0) {
        const unsigned z = ctl[0], zt = ctl[1];
        GAS unsigned char* sl = pace + (kslot & 63u) * 4352;
        if (half == 0u) {
          if (pub && zt != 0u && lane == 0) __hip_atomic_store((gu64*)(sl + (blk / PSTR) * 8), ((unsigned long long)zt << 32) | z, RLX_AGENT);
          half = 1;
        } else {
          const unsigned long long pv = __hip_atomic_load((gu64*)(sl + (lane < NPUB ? lane : 0) * 8), RLX_AGENT);
          const unsigned pz = (unsigned)pv, pt = (unsigned)(pv >> 32);
          const bool ok = (now - pt) < 1000u && (pz - z + 1000u) < 2000u && lane < NPUB;
          const float e = xsum(sum8(ok ? (float)(int)(pz - z) : 0.f)), n = xsum(sum8(ok ? 1.f : 0.f)), a = xsum(sum8(ok ? (float)(int)(now - pt) : 0.f));
          if (zt != 0u && n >= 0.5f * NPUB) {
            const float rn = __builtin_amdgcn_rcpf(n);
            const unsigned zr = z + (int)(e * rn), zrt = now - (int)(a * rn);
            float slope = (zrt - pzt) < 2000u ? (float)(int)(zr - pzr) * __builtin_amdgcn_rcpf((float)(zrt - pzt)) : 0.f;
            slope = fminf(fmaxf(slope * (SLP * 0.01f), -0.2f), 0.3f);
            pzr = zr; pzt = zrt;
            if (lane == 0) { ctl[2] = zr; ctl[3] = zrt; ctl[7] = __float_as_uint(slope); }
          }
          half = 0; kslot = now / PER + 1;
        }
      } else __builtin_amdgcn_s_sleep(4);
    }
  }
  const unsigned ka0 = lane >> 3, kb0 = 8u + ka0, ka1 = lane >> 4, kb1 = 4u + ka1;
  const bool vb0 = kb0 < nsl0, vb1 = kb1 < nsl1;
  const unsigned sa0 = SLOT0 + ka0 * SLOT, sb0 = SLOT0 + (vb0 ? kb0 : ka0) * SLOT, sa1 = SLOT0 + ka1 * SLOT, sb1 = SLOT0 + (vb1 ? kb1 : ka1) * SLOT;
  const unsigned in0 = (dim0 >> 2) * 16u + ((dim0 >> 1) & 1u) * 896u, in1 = (dim1 >> 2) * 16u + ((dim1 >> 1) & 1u) * 896u;
#pragma nounroll
  for (unsigned pass = (PM & 4) ? 0u : 1u; pass < 2u; ++pass) {
    const unsigned nrec = pass ? WSH : 0u;
    const auto w0r = __builtin_amdgcn_make_buffer_rsrc((void*)(ws + (size_t)h0 * WSH), 0, nrec, 0x00020000);
    const auto w1r = __builtin_amdgcn_make_buffer_rsrc((void*)(ws + (size_t)h1 * WSH), 0, nrec, 0x00020000);
    const auto o0r = __builtin_amdgcn_make_buffer_rsrc((void*)(out + h0 * D_), 0, pass ? ROWB : 0, 0x00020000);
    const auto o1r = __builtin_amdgcn_make_buffer_rsrc((void*)(out + h1 * D_), 0, pass ? ROWB : 0, 0x00020000);
    if (pass) {
#pragma nounroll
      for (unsigned spin = 0; spin < (1u << 24); ++spin) { const unsigned a = ctl[5], b = ctl[6]; if (a != 0u && b != 0u) break; }
      asm volatile("" ::: "memory");
      merge_publish(0, tag0, true);
      const float* recs = lds + L_REC + W * REC;
      const f32x2 ml0 = *reinterpret_cast<const f32x2*>(recs), ml1 = *reinterpret_cast<const f32x2*>(recs + REC);
      const float MM = fmaxf(ml0[0], ml1[0]), LL = fmaf(ex2(ml0[0] - MM), ml0[1], ex2(ml1[0] - MM) * ml1[1]);
      __builtin_amdgcn_raw_buffer_store_b128(u32x4{__float_as_uint(MM), tag1, __float_as_uint(LL), tag1}, w1r, lane == 0 ? 768 : OOB, SLOT0 + slot1 * SLOT, 16);
      if (PM >> 8) __builtin_amdgcn_s_sleep(PM >> 8);
      else {
#pragma nounroll
        for (unsigned spin = 0; spin < (1u << 24); ++spin) { if (ctl[10] >= ((PM & 2) ? 1u : 2u)) break; }
      }
    }
    bool timeout = false;
    float wa1, wb1, t1x, t1y;
    u32x4 xa, xb;
    STAMP(4);
    {
      u32x4 va, vb, ma, mb, na, nb;
#pragma nounroll
      for (unsigned spin = 0;; ++spin) {
        asm volatile("" ::: "memory");
        va = __builtin_amdgcn_raw_buffer_load_b128(w0r, sa0 + in0, 0, 16); vb = __builtin_amdgcn_raw_buffer_load_b128(w0r, sb0 + in0, 0, 16);
        ma = __builtin_amdgcn_raw_buffer_load_b128(w0r, sa0 + 768, 0, 16); mb = __builtin_amdgcn_raw_buffer_load_b128(w0r, sb0 + 768, 0, 16);
        na = __builtin_amdgcn_raw_buffer_load_b128(w1r, sa1 + 768, 0, 16); nb = __builtin_amdgcn_raw_buffer_load_b128(w1r, sb1 + 768, 0, 16);
        const unsigned bad = (va[1] ^ tag0) | (va[3] ^ tag0) | (vb[1] ^ tag0) | (vb[3] ^ tag0) | (ma[1] ^ tag0) | (ma[3] ^ tag0) | (mb[1] ^ tag0) | (mb[3] ^ tag0)
                           | (na[1] ^ tag1) | (na[3] ^ tag1) | (nb[1] ^ tag1) | (nb[3] ^ tag1);
        if (__all(bad == 0u) || pass == 0u) break;
        if (spin > (1u << 20)) { timeout = true; break; }
      }
      STAMP(3);
      if ((PM >> 8) && pass) {
#pragma nounroll
        for (unsigned spin = 0; spin < (1u << 24); ++spin) { if (ctl[10] >= 2u) break; }
      }
      asm volatile("" ::: "memory");
      xa = __builtin_amdgcn_raw_buffer_load_b128(w1r, sa1 + in1, 0, 16); xb = __builtin_amdgcn_raw_buffer_load_b128(w1r, sb1 + in1, 0, 16);
      asm volatile("" ::: "memory");
      __builtin_amdgcn_sched_barrier(0);
      {
        const float M = fmaxf(xmax(fmaxf(__uint_as_float(ma[0]), __uint_as_float(mb[0]))), sct0);
        const float wa = ex2(__uint_as_float(ma[0]) - M), wb = vb0 ? ex2(__uint_as_float(mb[0]) - M) : 0.f, wt = ex2(sct0 - M);
        const float L = xsum(fmaf(wa, __uint_as_float(ma[2]), wb * __uint_as_float(mb[2]))) + wt;
        const float x = xsum(fmaf(wa, __uint_as_float(va[0]), wb * __uint_as_float(vb[0]))), y = xsum(fmaf(wa, __uint_as_float(va[2]), wb * __uint_as_float(vb[2])));
        const float inv = timeout ? __builtin_nanf("") : __builtin_amdgcn_rcpf(L);
        __builtin_amdgcn_raw_buffer_store_b32(__builtin_bit_cast(unsigned, bf16x2{(__bf16)(fmaf(wt, bflo(vt0), x) * inv), (__bf16)(fmaf(wt, bfhi(vt0), y) * inv)}), o0r, lane < 8 ? dim0 * 2 : OOB, 0, 0);
        __builtin_amdgcn_raw_buffer_store_b32(tag0, w0r, (slot0 == 0u && lane == 0) ? 0 : OOB, 0, 16);
        __builtin_amdgcn_raw_buffer_store_b32(tag1, w1r, (slot1 == 0u && lane == 0) ? 0 : OOB, 0, 16);
      }
      {
        const float M = fmaxf(xmax(fmaxf(__uint_as_float(na[0]), __uint_as_float(nb[0]))), sct1);
        const float wa = ex2(__uint_as_float(na[0]) - M), wb = vb1 ? ex2(__uint_as_float(nb[0]) - M) : 0.f, wt = ex2(sct1 - M);
        const float inv = __builtin_amdgcn_rcpf(0.5f * xsum(fmaf(wa, __uint_as_float(na[2]), wb * __uint_as_float(nb[2]))) + wt);
        wa1 = wa * inv; wb1 = wb * inv; t1x = wt * inv * bflo(vt1); t1y = wt * inv * bfhi(vt1);
      }
    }
    asm volatile("" : "+v"(wa1), "+v"(wb1), "+v"(t1x), "+v"(t1y));
    STAMP(1);
    {
      u32x4 va = xa, vb = xb;
#pragma nounroll
      for (unsigned spin = 0;; ++spin) {
        unsigned bad = (va[1] ^ tag1) | (va[3] ^ tag1) | (vb[1] ^ tag1) | (vb[3] ^ tag1);
        if constexpr (DBG) { asm volatile("" : "+v"(bad)); if (nit_ == 0) STAMP(2); nit_ += pass; }
        if (__all(bad == 0u) || pass == 0u) break;
        if (spin > (1u << 20)) { timeout = true; break; }
        asm volatile("" ::: "memory");
        va = __builtin_amdgcn_raw_buffer_load_b128(w1r, sa1 + in1, 0, 16); vb = __builtin_amdgcn_raw_buffer_load_b128(w1r, sb1 + in1, 0, 16);
      }
      float x = fmaf(wa1, __uint_as_float(va[0]), wb1 * __uint_as_float(vb[0])), y = fmaf(wa1, __uint_as_float(va[2]), wb1 * __uint_as_float(vb[2]));
      { auto s1 = __builtin_amdgcn_permlane32_swap(__float_as_uint(x), __float_as_uint(y), false, false);
        float z = __uint_as_float(s1[0]) + __uint_as_float(s1[1]);
        auto s2 = __builtin_amdgcn_permlane16_swap(__float_as_uint(z), __float_as_uint(z), false, false);
        z = __uint_as_float(s2[0]) + __uint_as_float(s2[1]);
        auto s3 = __builtin_amdgcn_permlane32_swap(__float_as_uint(z), __float_as_uint(z), false, false);
        x = __uint_as_float(s3[0]) + t1x; y = __uint_as_float(s3[1]) + t1y; }
      if (timeout) x = __builtin_nanf("");
      __builtin_amdgcn_raw_buffer_store_b32(__builtin_bit_cast(unsigned, bf16x2{(__bf16)x, (__bf16)y}), o1r, lane < 16 ? dim1 * 2 : OOB, 0, 0);
    }
  }
  if constexpr (DBG) { STAMP(5); if (lane == 0) { unsigned long long* sp = (unsigned long long*)(ws + (64 << 20)) + (size_t)(blk * W) * 8; sp[4] = ts[4]; sp[5] = ts[5]; sp[8 + 4] = ts[4]; sp[8 + 5] = ts[5];
    unsigned long long* hp = (unsigned long long*)(ws + (80 << 20)) + (size_t)blk * 8; hp[0] = ts[4]; hp[1] = ts[4]; hp[7] = ts[3]; hp[2] = ts[2]; hp[3] = ts[5]; hp[4] = nit_; hp[5] = h0 == 42u ? 1 : (h1 == 42u ? 2 : 0); hp[6] = h1; } }
}
}

extern "C" void kernel_launch(void* const* d_in, const int* in_sizes, int n_in, void* d_out, int out_size, void* d_ws, size_t ws_size,
                              hipStream_t stream) {
  th::attn9<8,9,12,30,162,16,512,50,2,0><<<256, 192, 0, stream>>>((const unsigned char*)d_in[0], (const unsigned char*)d_in[1], (const unsigned char*)d_in[2], (const unsigned char*)d_in[3],
      (const unsigned char*)d_in[4], (const unsigned short*)d_in[5], (unsigned short*)d_out, (unsigned char*)d_ws);
}
```

```cpp
#include <hip/hip_runtime.h>
#include <hip/hip_bf16.h>
#include <stdint.h>

namespace ta {
constexpr int B_ = 2, H_ = 32, C_ = 8192, D_ = 192;
constexpr int BH = B_ * H_;
constexpr int ROWB = D_ * 2;
constexpr int TILEB = 8 * ROWB;
constexpr int HEADB = C_ * ROWB;
constexpr int NTILE = C_ / 8;
constexpr float LOG2E = 1.4426950408889634f;
constexpr float SCALE_LOG2E = 0.07216878364870322f * LOG2E;
constexpr int PREC = 196;
constexpr int WS_HEAD = 20736;
constexpr int WS_SLOT0 = 1024, WS_SLOT = 1792;

#define GAS __attribute__((address_space(1)))
typedef unsigned u32x4 __attribute__((ext_vector_type(4)));
typedef __bf16 bf16x2 __attribute__((ext_vector_type(2)));
typedef float f32x4 __attribute__((ext_vector_type(4)));
typedef float f32x2 __attribute__((ext_vector_type(2)));
typedef GAS unsigned gu32;
typedef GAS unsigned long long gu64;
#define RLX_AGENT __ATOMIC_RELAXED, __HIP_MEMORY_SCOPE_AGENT

__device__ __forceinline__ float dot2(unsigned a, unsigned b, float c) {
  return __builtin_amdgcn_fdot2_f32_bf16(__builtin_bit_cast(bf16x2, a), __builtin_bit_cast(bf16x2, b), c, false);
}
template <int CTRL> __device__ __forceinline__ float dpp(float x) {
  return __builtin_bit_cast(float, __builtin_amdgcn_mov_dpp(__builtin_bit_cast(int, x), CTRL, 0xf, 0xf, true));
}
constexpr int XOR1 = 0xB1, XOR2 = 0x4E, XOR7 = 0x141, XOR8 = 0x128;
__device__ __forceinline__ float sum8(float x) {
  x += dpp<XOR1>(x); x += dpp<XOR2>(x); x += dpp<XOR7>(x); return x;
}
__device__ __forceinline__ float xsum(float x) {
  x += dpp<XOR8>(x);
  auto s = __builtin_amdgcn_permlane16_swap(__float_as_uint(x), __float_as_uint(x), false, false);
  x = __uint_as_float(s[0]) + __uint_as_float(s[1]);
  auto t = __builtin_amdgcn_permlane32_swap(__float_as_uint(x), __float_as_uint(x), false, false);
  return __uint_as_float(t[0]) + __uint_as_float(t[1]);
}
__device__ __forceinline__ float xmax(float x) {
  x = fmaxf(x, dpp<XOR8>(x));
  auto s = __builtin_amdgcn_permlane16_swap(__float_as_uint(x), __float_as_uint(x), false, false);
  x = fmaxf(__uint_as_float(s[0]), __uint_as_float(s[1]));
  auto t = __builtin_amdgcn_permlane32_swap(__float_as_uint(x), __float_as_uint(x), false, false);
  return fmaxf(__uint_as_float(t[0]), __uint_as_float(t[1]));
}
__device__ __forceinline__ float bflo(unsigned u) { return __uint_as_float(u << 16); }
__device__ __forceinline__ float bfhi(unsigned u) { return __uint_as_float(u & 0xffff0000u); }
__device__ __forceinline__ float ex2(float x) { return __builtin_amdgcn_exp2f(x); }

struct State { float m, l; f32x4 a[6]; };

template <int T> struct Buf { u32x4 k[T][3], v[T][3]; };

template <int T>
__device__ __forceinline__ void load_tiles(Buf<T>& b, __amdgpu_buffer_rsrc_t ks, __amdgpu_buffer_rsrc_t vs, unsigned loff, unsigned so) {
#pragma unroll
  for (int t = 0; t < T; ++t)
#pragma unroll
    for (int j = 0; j < 3; ++j) b.k[t][j] = __builtin_amdgcn_raw_buffer_load_b128(ks, loff + t * TILEB + j * 128, so, 2);
#pragma unroll
  for (int t = 0; t < T; ++t)
#pragma unroll
    for (int j = 0; j < 3; ++j) b.v[t][j] = __builtin_amdgcn_raw_buffer_load_b128(vs, loff + t * TILEB + j * 128, so, 2);
  asm volatile("" ::: "memory");
  __builtin_amdgcn_sched_barrier(0);
}

template <int T>
__device__ __forceinline__ void compute(State& st, const Buf<T>& b, const u32x4 (&qv)[3]) {
  float sc[T];
  float mn = st.m;
#pragma unroll
  for (int t = 0; t < T; ++t) {
    float d0 = 0.f, d1 = 0.f, d2 = 0.f;
#pragma unroll
    for (int k = 0; k < 4; ++k) { d0 = dot2(b.k[t][0][k], qv[0][k], d0); d1 = dot2(b.k[t][1][k], qv[1][k], d1); d2 = dot2(b.k[t][2][k], qv[2][k], d2); }
    sc[t] = sum8((d0 + d1) + d2) * SCALE_LOG2E;
    mn = fmaxf(mn, sc[t]);
  }
  const float alpha = ex2(st.m - mn);
  st.m = mn;
  float l = st.l * alpha;
#pragma unroll
  for (int k = 0; k < 6; ++k) st.a[k] *= alpha;
#pragma unroll
  for (int t = 0; t < T; ++t) {
    const float p = ex2(sc[t] - mn);
    l += p;
#pragma unroll
    for (int j = 0; j < 3; ++j) {
      const f32x4 lo = {bflo(b.v[t][j][0]), bfhi(b.v[t][j][0]), bflo(b.v[t][j][1]), bfhi(b.v[t][j][1])};
      const f32x4 hi = {bflo(b.v[t][j][2]), bfhi(b.v[t][j][2]), bflo(b.v[t][j][3]), bfhi(b.v[t][j][3])};
      st.a[2 * j] = __builtin_elementwise_fma(f32x4{p, p, p, p}, lo, st.a[2 * j]);
      st.a[2 * j + 1] = __builtin_elementwise_fma(f32x4{p, p, p, p}, hi, st.a[2 * j + 1]);
    }
  }
  st.l = l;
  asm volatile("" : "+v"(st.m), "+v"(st.l), "+v"(st.a[0]), "+v"(st.a[1]), "+v"(st.a[2]), "+v"(st.a[3]), "+v"(st.a[4]), "+v"(st.a[5]) :: "memory");
  __builtin_amdgcn_sched_barrier(0);
}

template <int NS, int W, int T, int MODE, int NSTAT, int ILV, int DBG>
__global__ void __launch_bounds__(W * 64) attn_main(const unsigned char* __restrict__ q, const unsigned char* __restrict__ kc,
                                                    const unsigned char* __restrict__ vc, const unsigned char* __restrict__ kt,
                                                    const unsigned char* __restrict__ vt, const unsigned short* __restrict__ tm,
                                                    unsigned short* __restrict__ out, unsigned char* __restrict__ ws) {
  __shared__ __attribute__((aligned(16))) float lds[W * PREC];
  unsigned long long ts[6] = {0, 0, 0, 0, 0, 0};
#define STAMP(i) do { if constexpr (DBG) ts[i] = __builtin_amdgcn_s_memrealtime(); } while (0)
#define DUMP() do { if constexpr (DBG) { if ((threadIdx.x & 63) == 0) { unsigned long long* sp = (unsigned long long*)(ws + (64 << 20)) + (size_t)(blockIdx.x * W + (threadIdx.x >> 6)) * 8; \
    for (int i_ = 0; i_ < 6; ++i_) sp[i_] = ts[i_]; sp[6] = (unsigned)__builtin_amdgcn_s_getreg(20 | (3 << 11)) & 0xfu; sp[7] = nit_ | ((unsigned long long)((unsigned)__builtin_amdgcn_s_getreg(63492) >> 8 & 0xffu) << 32); } } } while (0)
  int nit_ = 0;
  STAMP(0);
  const int s = blockIdx.x % NS, bh = blockIdx.x / NS;
  const int tid = threadIdx.x, lane = tid & 63, wave = __builtin_amdgcn_readfirstlane(tid >> 6);
  const int r = lane >> 3, c = lane & 7;
  const unsigned loff = r * ROWB + c * 16;
  const auto ks = __builtin_amdgcn_make_buffer_rsrc((void*)(kc + (size_t)bh * HEADB), 0, HEADB, 0x00020000);
  const auto vs = __builtin_amdgcn_make_buffer_rsrc((void*)(vc + (size_t)bh * HEADB), 0, HEADB, 0x00020000);
  unsigned char* wsh = ws + (size_t)bh * WS_HEAD;
  constexpr int NSTEP_ALL = NTILE / (NS * W * 2 * T);
  constexpr int NST = (MODE == 2) ? NSTAT : NSTEP_ALL;
  constexpr unsigned STEPB = (ILV == 0) ? W * T * TILEB : NS * W * T * TILEB;
  unsigned so = (ILV == 0) ? (unsigned)(s * (NST * 2 * W * T) + wave * T) * TILEB : (unsigned)(s * W + wave) * T * TILEB;
  Buf<T> A, Bf;
  load_tiles<T>(A, ks, vs, loff, so);
  so += STEPB;
  u32x4 qv[3], ktv[3], vtv[3];
#pragma unroll
  for (int j = 0; j < 3; ++j) qv[j] = *reinterpret_cast<const u32x4*>(q + bh * ROWB + j * 128 + c * 16);
#pragma unroll
  for (int j = 0; j < 3; ++j) ktv[j] = *reinterpret_cast<const u32x4*>(kt + bh * ROWB + j * 128 + c * 16);
#pragma unroll
  for (int j = 0; j < 3; ++j) vtv[j] = *reinterpret_cast<const u32x4*>(vt + bh * ROWB + j * 128 + c * 16);
  const unsigned mk = tm[bh / H_];
  unsigned tag = 0;
  if constexpr (MODE >= 1) tag = __hip_atomic_load((gu32*)wsh, RLX_AGENT) * 0x9E3779B1u + 0x7F4A7C15u;
  asm volatile("" ::: "memory");
  __builtin_amdgcn_sched_barrier(0);
  load_tiles<T>(Bf, ks, vs, loff, so);
  so += STEPB;
  State st;
  {
    float d0 = 0.f, d1 = 0.f, d2 = 0.f;
#pragma unroll
    for (int k = 0; k < 4; ++k) { d0 = dot2(ktv[0][k], qv[0][k], d0); d1 = dot2(ktv[1][k], qv[1][k], d1); d2 = dot2(ktv[2][k], qv[2][k], d2); }
    const float sct = sum8((d0 + d1) + d2) * SCALE_LOG2E + (1.0f - bflo(mk)) * (-1.0e9f * LOG2E);
    const bool own = (s == 0) && (tid < 8);
    st.m = own ? sct : -INFINITY;
    st.l = own ? 1.f : 0.f;
#pragma unroll
    for (int j = 0; j < 3; ++j) {
      const u32x4 z = own ? vtv[j] : u32x4{0u, 0u, 0u, 0u};
      st.a[2 * j] = f32x4{bflo(z[0]), bfhi(z[0]), bflo(z[1]), bfhi(z[1])};
      st.a[2 * j + 1] = f32x4{bflo(z[2]), bfhi(z[2]), bflo(z[3]), bfhi(z[3])};
    }
  }
  if constexpr (MODE >= 1) tag = __builtin_amdgcn_readfirstlane(tag);
  gu64* ticket = (gu64*)(wsh + 256);
  if constexpr (MODE == 2) {
    if (s == 0 && tid == 0) __hip_atomic_store(ticket, (unsigned long long)tag << 32, RLX_AGENT);
  }
  asm volatile("" : "+v"(st.m), "+v"(st.l), "+v"(st.a[0]), "+v"(st.a[1]), "+v"(st.a[2]), "+v"(st.a[3]), "+v"(st.a[4]), "+v"(st.a[5]) :: "memory");
  __builtin_amdgcn_sched_barrier(0);
  STAMP(1);

  if constexpr (MODE != 2) {
    for (int it = 0; it < NST - 1; ++it) {
      compute<T>(st, A, qv);
      load_tiles<T>(A, ks, vs, loff, so);
      so += STEPB;
      compute<T>(st, Bf, qv);
      load_tiles<T>(Bf, ks, vs, loff, so);
      so += STEPB;
    }
    compute<T>(st, A, qv);
    compute<T>(st, Bf, qv);
  } else {
    static_assert(MODE != 2 || NST >= 2, "");
    constexpr unsigned DYN0 = NST * NS * W * 2 * T;
    constexpr unsigned NTK = (NTILE - DYN0) / (2 * T);
    unsigned long long tk = 0;
    { unsigned zero; asm volatile("v_mov_b32 %0, 0" : "=v"(zero)); ticket = (gu64*)((GAS char*)ticket + zero); }
#pragma nounroll
    for (int it = 0;; ++it) {
      compute<T>(st, A, qv);
      unsigned soA = so, soB = so + STEPB;
      so += 2 * STEPB;
      if (it >= NST - 1) {
        unsigned hi = __builtin_amdgcn_readfirstlane((unsigned)(tk >> 32)), n = __builtin_amdgcn_readfirstlane((unsigned)tk);
#pragma nounroll
        for (unsigned spin = 0; hi != tag; ++spin) {
          if (spin > (1u << 20)) { n = 0xffffu; break; }
          __builtin_amdgcn_s_sleep(8);
          if (lane == 0) tk = __hip_atomic_fetch_add(ticket, 1ull, RLX_AGENT);
          hi = __builtin_amdgcn_readfirstlane((unsigned)(tk >> 32)); n = __builtin_amdgcn_readfirstlane((unsigned)tk);
        }
        if (n >= NTK) { nit_ = it; break; }
        soA = (DYN0 + n * 2 * T) * TILEB; soB = soA + T * TILEB;
      }
      load_tiles<T>(A, ks, vs, loff, soA);
      compute<T>(st, Bf, qv);
      if (it >= NST - 2 && lane == 0) tk = __hip_atomic_fetch_add(ticket, 1ull, RLX_AGENT);
      asm volatile("" ::: "memory");
      load_tiles<T>(Bf, ks, vs, loff, soB);
    }
    compute<T>(st, Bf, qv);
  }
  STAMP(2);
  const float M = xmax(st.m);
  const float w = ex2(st.m - M);
  const float l = xsum(st.l * w);
#pragma unroll
  for (int k = 0; k < 6; ++k)
#pragma unroll
    for (int e = 0; e < 4; ++e) st.a[k][e] = xsum(st.a[k][e] * w);
  float* rec = lds + wave * PREC;
  if (r == 0) {
#pragma unroll
    for (int j = 0; j < 3; ++j) {
      *reinterpret_cast<f32x4*>(rec + 4 + j * 64 + c * 8) = st.a[2 * j];
      *reinterpret_cast<f32x4*>(rec + 4 + j * 64 + c * 8 + 4) = st.a[2 * j + 1];
    }
    if (c == 0) { rec[0] = M; rec[1] = l; }
  }
  STAMP(3);
  __syncthreads();
  STAMP(4);
  if (W * 64 > D_ && tid >= D_) { DUMP(); return; }
  float MM = lds[0];
#pragma unroll
  for (int w2 = 1; w2 < W; ++w2) MM = fmaxf(MM, lds[w2 * PREC]);
  float L = 0.f, o = 0.f;
#pragma unroll
  for (int w2 = 0; w2 < W; ++w2) {
    const float wt = ex2(lds[w2 * PREC] - MM);
    L = fmaf(wt, lds[w2 * PREC + 1], L);
    o = fmaf(wt, lds[w2 * PREC + 4 + tid], o);
  }
  if constexpr (MODE == 0) {
    float* pr = (float*)(wsh + WS_SLOT0 + s * WS_SLOT);
    pr[4 + tid] = o;
    if (tid == 0) { pr[0] = MM; pr[1] = L; }
    STAMP(5); DUMP();
  } else {
    const unsigned long long th = (unsigned long long)tag << 32;
    if (s != 0) {
      gu64* g = (gu64*)(wsh + WS_SLOT0 + s * WS_SLOT);
      __hip_atomic_store(g + 2 + tid, th | __float_as_uint(o), RLX_AGENT);
      if (tid < 2) __hip_atomic_store(g + tid, th | __float_as_uint(tid == 0 ? MM : L), RLX_AGENT);
      STAMP(5); DUMP();
      return;
    }
    float pm[NS], pl[NS], po[NS];
    pm[0] = MM; pl[0] = L; po[0] = o;
#pragma nounroll
    for (unsigned spin = 0;; ++spin) {
      bool ok = true;
#pragma unroll
      for (int k = 1; k < NS; ++k) {
        gu64* g = (gu64*)(wsh + WS_SLOT0 + k * WS_SLOT);
        const unsigned long long x0 = __hip_atomic_load(g, RLX_AGENT), x1 = __hip_atomic_load(g + 1, RLX_AGENT), x2 = __hip_atomic_load(g + 2 + tid, RLX_AGENT);
        pm[k] = __uint_as_float((unsigned)x0); pl[k] = __uint_as_float((unsigned)x1); po[k] = __uint_as_float((unsigned)x2);
        ok &= ((unsigned)(x0 >> 32) == tag) & ((unsigned)(x1 >> 32) == tag) & ((unsigned)(x2 >> 32) == tag);
      }
      if (__all(ok)) break;
      if (spin > (1u << 20)) { pl[0] = __builtin_nanf(""); break; }
      __builtin_amdgcn_s_sleep(1);
    }
    float M2 = pm[0];
#pragma unroll
    for (int k = 1; k < NS; ++k) M2 = fmaxf(M2, pm[k]);
    float L2 = 0.f, o2 = 0.f;
#pragma unroll
    for (int k = 0; k < NS; ++k) { const float wt = ex2(pm[k] - M2); L2 = fmaf(wt, pl[k], L2); o2 = fmaf(wt, po[k], o2); }
    out[bh * D_ + tid] = __builtin_bit_cast(unsigned short, (__bf16)(o2 / L2));
    if (tid == 0) __hip_atomic_store((gu32*)wsh, tag, RLX_AGENT);
    STAMP(5); DUMP();
  }
}

template <int NS>
__global__ void __launch_bounds__(192) attn_combine(const unsigned char* __restrict__ ws, unsigned short* __restrict__ out) {
  const int bh = blockIdx.x, tid = threadIdx.x;
  float ms[NS], ls[NS], os[NS];
#pragma unroll
  for (int s = 0; s < NS; ++s) { const float* pr = (const float*)(ws + (size_t)bh * WS_HEAD + WS_SLOT0 + s * WS_SLOT); ms[s] = pr[0]; ls[s] = pr[1]; os[s] = pr[4 + tid]; }
  float M = ms[0];
#pragma unroll
  for (int s = 1; s < NS; ++s) M = fmaxf(M, ms[s]);
  float L = 0.f, o = 0.f;
#pragma unroll
  for (int s = 0; s < NS; ++s) { const float w = ex2(ms[s] - M); L = fmaf(w, ls[s], L); o = fmaf(w, os[s], o); }
  out[bh * D_ + tid] = __builtin_bit_cast(unsigned short, (__bf16)(o / L));
}
}

#define LAS __attribute__((address_space(3)))
namespace th {
using namespace ta;
typedef short bf16x8 __attribute__((ext_vector_type(8)));
typedef unsigned u32x2 __attribute__((ext_vector_type(2)));
typedef const __attribute__((address_space(4))) unsigned cu32;
constexpr int WSH = 20736, SLOT0 = 256, SLOT = 1664;
constexpr int NB = 8, W = 2, SR = 88 + 12, REC = 2 * D_ + 4;
constexpr int L_SC = 0, L_REC = W * 8 * SR, L_CTL = L_REC + 2 * W * REC, L_END = L_CTL + 16;
constexpr unsigned OOB = 0x40000000u;
template <int V> struct IC { static constexpr int value = V; };

__device__ __forceinline__ unsigned unit_entry(unsigned h, unsigned cls, unsigned n) {
  const unsigned P = n >> 5, y = n & 31u, S = 48u * h + 3u * P;
  const unsigned f0 = (cls ^ (S >> 3) ^ (S >> 10)) & 1u, f1 = (cls ^ ((S + 1) >> 3) ^ ((S + 1) >> 10)) & 1u, f2 = (cls ^ ((S + 2) >> 3) ^ ((S + 2) >> 10)) & 1u;
  unsigned o0 = f0 * 11u, o1 = 31u + f0 + f1 * (11u - f0 + f2), o2 = 64u + f2 * 11u;
  asm volatile("" : "+v"(o0), "+v"(o1), "+v"(o2));
  const bool a = y < 11u - f0, b = y < 22u - f2;
  return (128u * P + y + (a ? o0 : (b ? o1 : o2))) * (4u * ROWB) + ((!a && b) ? 1u : 0u);
}

template <int THL, int TH, int THH, int DELTA, int TAU, int NPUB, int PER, int SLP, int PM, int DBG, int NPRE = (TH + 3) / 3, int ENDG = 0, int ENDB = 0, int LA = 16, int SA = 16, int TABL = -1>
__global__ void __launch_bounds__(192) attn9(const unsigned char* __restrict__ q, const unsigned char* __restrict__ kc,
                                             const unsigned char* __restrict__ vc, const unsigned char* __restrict__ kt,
                                             const unsigned char* __restrict__ vt, const unsigned short* __restrict__ tm,
                                             unsigned short* __restrict__ out, unsigned char* __restrict__ ws) {
  __shared__ __attribute__((aligned(16))) float lds[L_END];
  unsigned long long ts[6] = {0, 0, 0, 0, 0, 0};
  int nit_ = 0, n1_ = 0;
  STAMP(0);
  __builtin_amdgcn_sched_barrier(0);
  const unsigned blk = blockIdx.x;
  const int tid = threadIdx.x, lane = tid & 63, wave = __builtin_amdgcn_readfirstlane(tid >> 6);
  const int r = lane >> 3, c = lane & 7;
  const unsigned cls = (blk >> 2) & 1u, ci = (blk >> 3) * 4u + (blk & 3u);
  const unsigned h1 = (ci * 171u) >> 9, mA = ci - 3u * h1;
  const unsigned hq = ((ci + 4u) * 171u) >> 10, h0 = 42u + hq, mB = ci + 4u - 6u * hq;
  const unsigned n0 = 40u + (mA & 1u) * 8u;
  const unsigned st0 = mB * 80u + ((mB + 1u) >> 2) * 16u + wave, st1 = mA * 176u - (mA >> 1) * 16u + wave - 2u * n0;
  unsigned te0;
  { const bool g = (unsigned)lane >= n0; te0 = unit_entry(g ? h1 : h0, cls, (g ? st1 : st0) + 2u * lane); }
  if constexpr (TABL >= 0) te0 = ((const unsigned*)(ws + (48 << 20)))[(blk * 2 + (wave & 1)) * 128 + lane];
  const unsigned lbase = (r & ((1 << (TABL & 3)) - 1)) * ROWB + c * 16, lhi = (r >> (TABL & 3)) * ROWB;
  const unsigned vo84 = (r & 3) * ROWB + c * 16 + (r >> 2) * (84 * ROWB), vstep = (r >> 2) * (4 * ROWB);
  volatile LAS unsigned* ctl = (volatile LAS unsigned*)(lds + L_CTL);
  const unsigned slot1 = cls * (h1 == 42u ? 2u : 3u) + mA, slot0 = cls * (h0 == 42u ? 2u : 6u) + mB;
  const int l4 = (lane < 48 ? lane : 0) * 4;
  auto merge_publish = [&](int g, unsigned tag, bool with_ml) {
    const float* recs = lds + L_REC + g * W * REC;
    const f32x2 ml0 = *reinterpret_cast<const f32x2*>(recs), ml1 = *reinterpret_cast<const f32x2*>(recs + REC);
    const f32x4 a0 = *reinterpret_cast<const f32x4*>(recs + 4 + l4), a1 = *reinterpret_cast<const f32x4*>(recs + 4 + D_ + l4);
    const f32x4 b0 = *reinterpret_cast<const f32x4*>(recs + REC + 4 + l4), b1 = *reinterpret_cast<const f32x4*>(recs + REC + 4 + D_ + l4);
    const float MM = fmaxf(ml0[0], ml1[0]), w0 = ex2(ml0[0] - MM), w1 = ex2(ml1[0] - MM);
    const f32x4 o = w0 * (a0 + a1) + w1 * (b0 + b1);
    const auto wr = __builtin_amdgcn_make_buffer_rsrc((void*)(ws + (size_t)(g ? h1 : h0) * WSH), 0, WSH, 0x00020000);
    const unsigned so = SLOT0 + (g ? slot1 : slot0) * SLOT;
    const u32x4 g0 = lane < 48 ? u32x4{__float_as_uint(o[0]), tag, __float_as_uint(o[1]), tag} : u32x4{__float_as_uint(MM), tag, __float_as_uint(fmaf(w0, ml0[1], w1 * ml1[1])), tag};
    __builtin_amdgcn_raw_buffer_store_b128(g0, wr, (with_ml ? lane <= 48 : lane < 48) ? lane * 16 : OOB, so, SA);
    __builtin_amdgcn_raw_buffer_store_b128(u32x4{__float_as_uint(o[2]), tag, __float_as_uint(o[3]), tag}, wr, lane < 48 ? lane * 16 + 896 : OOB, so, SA);
  };
  asm volatile("" :: "s"(q), "s"(kc), "s"(vc), "v"(te0));

  if (__builtin_expect(wave < W, 1)) {
    unsigned te1 = 0, tpre = 0;
    if constexpr (TABL >= 0) { tpre = ((const unsigned*)(ws + (48 << 20)))[(blk * 2 + wave) * 128 + 64 + lane]; asm volatile("" : "+v"(tpre)); }
    u32x4 buf[NB][3];
    struct Addr { unsigned so, vo; };
    auto prep = [&](unsigned tsel, int e) {
      const unsigned x = __builtin_amdgcn_readlane(tsel, e);
      Addr a;
      if constexpr (TABL >= 0) { a.so = (x & 8191u) * ROWB; a.vo = lbase + lhi * (x >> 13); }
      else { a.so = x & ~1u; a.vo = vo84 + vstep * (x & 1u); }
      asm volatile("" : "+s"(a.so));
      asm volatile("" : "+v"(a.vo));
      __builtin_amdgcn_sched_barrier(0);
      return a;
    };
    auto issue = [&](u32x4 (&dst)[3], __amdgpu_buffer_rsrc_t rs, const Addr& a, auto mode, int on) {
      if constexpr (mode.value == 1) asm volatile("s_waitcnt vmcnt(%0)" :: "i"(TH) : "memory");
      if constexpr (mode.value == 3) asm volatile("s_waitcnt vmcnt(%0)" :: "i"(3 * NPRE + 3) : "memory");
      if constexpr (mode.value == 2) asm volatile("s_cmp_lt_i32 %3, 0\n\ts_waitcnt vmcnt(%0)\n\ts_cbranch_scc1 1f\n\ts_cmp_le_i32 %3, %4\n\ts_waitcnt vmcnt(%1)\n\ts_cbranch_scc1 1f\n\ts_waitcnt vmcnt(%2)\n1:"
                                                  :: "i"(THH), "i"(TH), "i"(THL), "s"(on), "i"(2 * DELTA) : "scc", "memory");
#pragma unroll
      for (int j = 0; j < 3; ++j) dst[j] = __builtin_amdgcn_raw_buffer_load_b128(rs, a.vo + j * 128, a.so, 2);
      asm volatile("" ::: "memory");
      __builtin_amdgcn_sched_barrier(0);
    };
    auto rsrc = [&](const unsigned char* p, unsigned h, unsigned bytes) { return __builtin_amdgcn_make_buffer_rsrc((void*)(p + (size_t)h * HEADB), 0, bytes, 0x00020000); };
    u32x4 qv[3], qn[3];
    {
      const auto rs = rsrc(kc, h0, HEADB);
      if (DBG) STAMP(4);
#pragma unroll
      for (int b = 0; b < NPRE; ++b) issue(buf[b], rs, prep(te0, b), IC<0>{}, 0);
#pragma unroll
      for (int j = 0; j < 3; ++j) qv[j] = *reinterpret_cast<const u32x4*>(q + h0 * ROWB + j * 128 + c * 16);
#pragma unroll
      for (int j = 0; j < 3; ++j) qn[j] = *reinterpret_cast<const u32x4*>(q + h1 * ROWB + j * 128 + c * 16);
      asm volatile("" ::: "memory");
      __builtin_amdgcn_sched_barrier(0);
      if (DBG) STAMP(5);
      te1 = unit_entry(h1, cls, st1 + 128u + 2u * lane);
      if constexpr (TABL >= 0) te1 = tpre;
      asm volatile("" : "+v"(te1));
      __builtin_amdgcn_s_barrier();
      __builtin_amdgcn_sched_barrier(0);
#pragma unroll
      for (int b = NPRE; b < NB; ++b) { if (b < 2 * NPRE) issue(buf[b], rs, prep(te0, b), IC<3>{}, 0); else issue(buf[b], rs, prep(te0, b), IC<1>{}, 0); }
    }
    STAMP(1);
    int err = DELTA;
    unsigned rt = 0, re = 0, tagw = 0;
    unsigned prog = 0;
    const unsigned zoff = h1 == 42u ? 60u : 0u;
    auto pace_step = [&]() {
      const unsigned zr = ctl[2], zt = ctl[3]; const float sl = __uint_as_float(ctl[7]);
      const unsigned now = (unsigned)__builtin_amdgcn_s_memrealtime();
      const unsigned z = now - prog + zoff;
      if (tid == 0) { ctl[0] = z; ctl[1] = now; if (prog == 29u * TAU) ctl[4] = 1u; }
      if constexpr (DBG) { rt = (unsigned)lane == prog / TAU ? now : rt; re = (unsigned)lane == prog / TAU ? (unsigned)err : re; }
      prog += TAU;
      int ei = (now - zt) < 1500u ? (int)(zr - z) + (int)(sl * (float)(now - zt)) + DELTA : DELTA;
      asm volatile("" : "+v"(ei));
      err = __builtin_amdgcn_readfirstlane(ei);
      asm volatile("" ::: "memory");
      __builtin_amdgcn_sched_barrier(0);
    };
    float* sc = lds + L_SC + wave * 8 * SR + r * SR;
    const unsigned* pu = reinterpret_cast<const unsigned*>(sc);
    u32x4 msk;
#pragma unroll
    for (int i = 0; i < 4; ++i) msk[i] = (c >> 1) == i ? ((c & 1) ? 0xffff0000u : 0x0000ffffu) : 0u;
#pragma nounroll
    for (int g = 0; g < 2; ++g) {
      const int n = g ? 128 - n0 : n0, e0 = g ? n0 : 0;
      const unsigned h = g ? h1 : h0;
      float mx = -INFINITY;
#pragma nounroll
      for (int k = 0; k < n; k += NB) {
        pace_step();
        const bool last = k + NB == n;
        const auto rs = rsrc(last ? vc : kc, h, HEADB);
        const int en = last ? e0 : e0 + k + NB;
        const unsigned tsel = (en & 64) ? te1 : te0;
        float d[NB];
#pragma unroll
        for (int b = 0; b < NB; ++b) {
          const Addr nx = prep(tsel, (en & 63) + b);
          float d0 = 0.f, d1 = 0.f, d2 = 0.f;
#pragma unroll
          for (int e = 0; e < 4; ++e) { d0 = dot2(buf[b][0][e], qv[0][e], d0); d1 = dot2(buf[b][1][e], qv[1][e], d1); d2 = dot2(buf[b][2][e], qv[2][e], d2); }
          asm volatile("" : "+v"(d0), "+v"(d1), "+v"(d2) :: "memory");
          __builtin_amdgcn_sched_barrier(0);
          issue(buf[b], rs, nx, IC<2>{}, err);
          d[b] = sum8((d0 + d1) + d2);
          mx = fmaxf(mx, d[b]);
          if ((b & 3) == 3 && c == 0) *reinterpret_cast<f32x4*>(sc + k + b - 3) = f32x4{d[b - 3], d[b - 2], d[b - 1], d[b]};
        }
      }
      const float Mw = xmax(mx) * SCALE_LOG2E;
      float lsum = 0.f;
      __builtin_amdgcn_fence(__ATOMIC_ACQ_REL, "wavefront");
      {
        f32x4 x[3];
#pragma unroll
        for (int e = 0; e < 3; ++e) x[e] = *reinterpret_cast<const f32x4*>(sc + e * 32 + c * 4);
#pragma unroll
        for (int e = 0; e < 3; ++e) {
          const bool in = e * 32 + c * 4 < n;
          u32x4 y;
#pragma unroll
          for (int i = 0; i < 4; ++i) {
            const float p = in ? ex2(fmaf(x[e][i], SCALE_LOG2E, -Mw)) : 0.f;
            y[i] = __builtin_bit_cast(unsigned, bf16x2{(__bf16)p, (__bf16)p});
            lsum += bfhi(y[i]);
          }
          *reinterpret_cast<u32x4*>(sc + e * 32 + c * 4) = y;
        }
      }
      __builtin_amdgcn_fence(__ATOMIC_ACQ_REL, "wavefront");
      lsum = xsum(sum8(lsum));
      float* rec = lds + L_REC + (g * W + wave) * REC;
      if (lane == 0) *reinterpret_cast<f32x2*>(rec) = f32x2{Mw, lsum};
      f32x4 acc[3] = {{0.f, 0.f, 0.f, 0.f}, {0.f, 0.f, 0.f, 0.f}, {0.f, 0.f, 0.f, 0.f}};
      const int nv = g ? n - NB : n;
#pragma nounroll
      for (int k = 0; k < nv; k += NB) {
        const u32x4 pa = *reinterpret_cast<const u32x4*>(pu + k), pb = *reinterpret_cast<const u32x4*>(pu + k + 4);
        pace_step();
        if ((PM & 16) && prog == 31u * TAU && lane == 0) ctl[5 + wave] = 1u;
        if (ENDG && prog > (31u - ENDG) * TAU) err -= ENDB;
        const bool last = k + NB == n;
        const auto rs = rsrc(last ? kc : vc, last ? h1 : h, HEADB);
        const int en = e0 + k + NB;
        const unsigned tsel = (en & 64) ? te1 : te0;
#pragma unroll
        for (int b = 0; b < NB; ++b) {
          const Addr nx = prep(tsel, (en & 63) + b);
          const unsigned p = b < 4 ? pa[b & 3] : pb[b & 3];
          const u32x4 a = {p & msk[0], p & msk[1], p & msk[2], p & msk[3]};
#pragma unroll
          for (int j = 0; j < 3; ++j) acc[j] = __builtin_amdgcn_mfma_f32_16x16x32_bf16(__builtin_bit_cast(bf16x8, a), __builtin_bit_cast(bf16x8, buf[b][j]), acc[j], 0, 0, 0);
          asm volatile("" : "+a"(acc[0]), "+a"(acc[1]), "+a"(acc[2]) :: "memory");
          __builtin_amdgcn_sched_barrier(0);
          issue(buf[b], rs, nx, IC<2>{}, err);
          if ((PM & 32) && b == 4 && prog == 31u * TAU && lane == 0) ctl[5 + wave] = 1u;
        }
      }
      if (g) {
        const u32x4 pa = *reinterpret_cast<const u32x4*>(pu + nv), pb = *reinterpret_cast<const u32x4*>(pu + nv + 4);
        if constexpr (DBG) pace_step();
        if (lane == 0) ctl[5 + wave] = 1u;
        for (unsigned spin = 0; ctl[9] == 0u && spin < (1u << 24); ++spin) {}
        tagw = ctl[8];
        asm volatile("" : "+v"(tagw) :: "memory");
#pragma unroll
        for (int b = 0; b < NB; ++b) {
          const unsigned p = b < 4 ? pa[b & 3] : pb[b & 3];
          const u32x4 a = {p & msk[0], p & msk[1], p & msk[2], p & msk[3]};
#pragma unroll
          for (int j = 0; j < 3; ++j) acc[j] = __builtin_amdgcn_mfma_f32_16x16x32_bf16(__builtin_bit_cast(bf16x8, a), __builtin_bit_cast(bf16x8, buf[b][j]), acc[j], 0, 0, 0);
        }
      }
      if (g) STAMP(2);
      {
        const int hh = lane >> 4, nn = lane & 15;
        if ((hh >> 1) == (nn >> 3)) {
#pragma unroll
          for (int j = 0; j < 3; ++j) *reinterpret_cast<f32x4*>(rec + 4 + (hh >> 1) * D_ + j * 64 + (nn & 7) * 8 + (hh & 1) * 4) = acc[j];
        }
      }
#pragma unroll
      for (int j = 0; j < 3; ++j) qv[j] = qn[j];
    }
    {
      asm volatile("" ::: "memory");
      unsigned old = 0;
      if (lane == 0) old = __hip_atomic_fetch_add((LAS unsigned*)(lds + L_CTL + 10), 1u, __ATOMIC_RELAXED, __HIP_MEMORY_SCOPE_WORKGROUP);
      old = __builtin_amdgcn_readfirstlane(old);
      asm volatile("" ::: "memory");
      if (old == 1u) merge_publish(1, tagw, false);
    }
    if constexpr (DBG) { unsigned* rp = (unsigned*)(ws + (72 << 20)) + (size_t)(blk * W + wave) * 128; rp[lane] = rt; rp[64 + lane] = re; }
    if constexpr (DBG) { STAMP(3); if (lane == 0) { unsigned long long* sp = (unsigned long long*)(ws + (64 << 20)) + (size_t)(blk * W + wave) * 8;
      for (int i_ = 0; i_ < 4; ++i_) sp[i_] = ts[i_]; sp[6] = (unsigned)__builtin_amdgcn_s_getreg(20 | (3 << 11)) & 0xfu; sp[7] = 0; } }
    return;
  }

  if (lane < 16) ctl[lane] = 0u;
  asm volatile("s_waitcnt lgkmcnt(0)" ::: "memory");
  __builtin_amdgcn_s_barrier();
  constexpr unsigned PSTR = 256 / NPUB + 1;
  const bool pub = (blk % PSTR) == 0u && (blk / PSTR) < (unsigned)NPUB;
  GAS unsigned char* pace = (GAS unsigned char*)(ws + (size_t)BH * WSH);
  const unsigned nsl0 = h0 == 42u ? 8u : 12u, nsl1 = h1 == 42u ? 8u : 6u;
  const unsigned dim0 = 16u * slot0 + (h0 == 42u ? 64u : 0u) + 4u * (lane & 3) + 2u * ((lane >> 2) & 1);
  const unsigned dim1 = 32u * slot1 + 4u * (lane & 7) + 2u * ((lane >> 3) & 1);
  unsigned tag0, tag1;
  float sct0, sct1; unsigned vt0, vt1;
  {
    const unsigned e0 = *(cu32*)(ws + (size_t)h0 * WSH), e1 = *(cu32*)(ws + (size_t)h1 * WSH), mk2 = *(cu32*)tm;
    if (PM & 8) {
      const unsigned a0 = __builtin_amdgcn_readlane(te0, 0) & ~3u, a1 = __builtin_amdgcn_readlane(te0, 36) & ~3u, b0 = __builtin_amdgcn_readlane(te0, 48) & ~3u;
      const unsigned b1 = min(b0 + (800u << 10), (unsigned)HEADB - 4u);
      const unsigned char* k0 = kc + (size_t)h0 * HEADB; const unsigned char* v0 = vc + (size_t)h0 * HEADB;
      const unsigned char* k1 = kc + (size_t)h1 * HEADB; const unsigned char* v1 = vc + (size_t)h1 * HEADB;
      unsigned w0 = *(cu32*)(k0 + a1), w1 = *(cu32*)(v0 + a0), w2 = *(cu32*)(v0 + a1), w3 = *(cu32*)(k1 + b0), w4 = *(cu32*)(k1 + b1), w5 = *(cu32*)(v1 + b0), w6 = *(cu32*)(v1 + b1), w7 = *(cu32*)(out + h0 * D_);
      asm volatile("" :: "s"(w0), "s"(w1), "s"(w2), "s"(w3), "s"(w4), "s"(w5), "s"(w6), "s"(w7) : "memory");
    }
    if (PM & 1) {
      unsigned w0 = *(cu32*)(kt + h0 * ROWB), w1 = *(cu32*)(vt + h0 * ROWB), w2 = *(cu32*)(out + h0 * D_);
      asm volatile("" :: "s"(w0), "s"(w1), "s"(w2) : "memory");
      __builtin_amdgcn_s_sleep(127);
      asm volatile("" ::: "memory");
    }
    const unsigned hh = lane < 32 ? h0 : h1;
    float d0 = 0.f;
#pragma unroll
    for (int j = 0; j < 3; ++j) {
      const u32x4 a = *reinterpret_cast<const u32x4*>(q + hh * ROWB + j * 128 + c * 16), b = *reinterpret_cast<const u32x4*>(kt + hh * ROWB + j * 128 + c * 16);
#pragma unroll
      for (int k = 0; k < 4; ++k) d0 = dot2(a[k], b[k], d0);
    }
    vt0 = *reinterpret_cast<const unsigned*>(vt + h0 * ROWB + dim0 * 2);
    vt1 = *reinterpret_cast<const unsigned*>(vt + h1 * ROWB + dim1 * 2);
    const float sc2 = sum8(d0) * SCALE_LOG2E + (1.0f - (hh >= H_ ? bfhi(mk2) : bflo(mk2))) * (-1.0e9f * LOG2E);
    sct0 = __uint_as_float(__builtin_amdgcn_readlane(__float_as_uint(sc2), 0));
    sct1 = __uint_as_float(__builtin_amdgcn_readlane(__float_as_uint(sc2), 32));
    tag0 = e0 * 0x9E3779B1u + 0x7F4A7C15u;
    tag1 = e1 * 0x9E3779B1u + 0x7F4A7C15u;
    if (lane == 0) { ctl[8] = tag1; ctl[9] = 1u; }
  }
  asm volatile("" : "+v"(vt0), "+v"(vt1) :: "memory");
  {
    unsigned kslot = (unsigned)__builtin_amdgcn_s_memrealtime() / PER + 1, half = 0, pzr = 0, pzt = 0;
#pragma nounroll
    for (unsigned spin = 0; spin < (1u << 24); ++spin) {
      if (ctl[4] != 0u) break;
      const unsigned now = (unsigned)__builtin_amdgcn_s_memrealtime();
      if ((int)(now - (kslot * PER + half * (PER / 2))) >= 0) {
        const unsigned z = ctl[0], zt = ctl[1];
        GAS unsigned char* sl = pace + (kslot & 63u) * 4352;
        if (half == 0u) {
          if (pub && zt != 0u && lane == 0) __hip_atomic_store((gu64*)(sl + (blk / PSTR) * 8), ((unsigned long long)zt << 32) | z, RLX_AGENT);
          half = 1;
        } else {
          const unsigned long long pv = __hip_atomic_load((gu64*)(sl + (lane < NPUB ? lane : 0) * 8), RLX_AGENT);
          const unsigned pz = (unsigned)pv, pt = (unsigned)(pv >> 32);
          const bool ok = (now - pt) < 1000u && (pz - z + 1000u) < 2000u && lane < NPUB;
          const float e = xsum(sum8(ok ? (float)(int)(pz - z) : 0.f)), n = xsum(sum8(ok ? 1.f : 0.f)), a = xsum(sum8(ok ? (float)(int)(now - pt) : 0.f));
          if (zt != 0u && n >= 0.5f * NPUB) {
            const float rn = __builtin_amdgcn_rcpf(n);
            const unsigned zr = z + (int)(e * rn), zrt = now - (int)(a * rn);
            float slope = (zrt - pzt) < 2000u ? (float)(int)(zr - pzr) * __builtin_amdgcn_rcpf((float)(zrt - pzt)) : 0.f;
            slope = fminf(fmaxf(slope * (SLP * 0.01f), -0.2f), 0.3f);
            pzr = zr; pzt = zrt;
            if (lane == 0) { ctl[2] = zr; ctl[3] = zrt; ctl[7] = __float_as_uint(slope); }
          }
          half = 0; kslot = now / PER + 1;
        }
      } else __builtin_amdgcn_s_sleep(4);
    }
  }
  const unsigned ka0 = lane >> 3, kb0 = 8u + ka0, ka1 = lane >> 4, kb1 = 4u + ka1;
  const bool vb0 = kb0 < nsl0, vb1 = kb1 < nsl1;
  const unsigned sa0 = SLOT0 + ka0 * SLOT, sb0 = SLOT0 + (vb0 ? kb0 : ka0) * SLOT, sa1 = SLOT0 + ka1 * SLOT, sb1 = SLOT0 + (vb1 ? kb1 : ka1) * SLOT;
  const unsigned in0 = (dim0 >> 2) * 16u + ((dim0 >> 1) & 1u) * 896u, in1 = (dim1 >> 2) * 16u + ((dim1 >> 1) & 1u) * 896u;
#pragma nounroll
  for (unsigned pass = (PM & 4) ? 0u : 1u; pass < 2u; ++pass) {
    const unsigned nrec = pass ? WSH : 0u;
    const auto w0r = __builtin_amdgcn_make_buffer_rsrc((void*)(ws + (size_t)h0 * WSH), 0, nrec, 0x00020000);
    const auto w1r = __builtin_amdgcn_make_buffer_rsrc((void*)(ws + (size_t)h1 * WSH), 0, nrec, 0x00020000);
    const auto o0r = __builtin_amdgcn_make_buffer_rsrc((void*)(out + h0 * D_), 0, pass ? ROWB : 0, 0x00020000);
    const auto o1r = __builtin_amdgcn_make_buffer_rsrc((void*)(out + h1 * D_), 0, pass ? ROWB : 0, 0x00020000);
    if (pass) {
#pragma nounroll
      for (unsigned spin = 0; spin < (1u << 24); ++spin) { const unsigned a = ctl[5], b = ctl[6]; if (a != 0u && b != 0u) break; }
      asm volatile("" ::: "memory");
      merge_publish(0, tag0, true);
      const float* recs = lds + L_REC + W * REC;
      const f32x2 ml0 = *reinterpret_cast<const f32x2*>(recs), ml1 = *reinterpret_cast<const f32x2*>(recs + REC);
      const float MM = fmaxf(ml0[0], ml1[0]), LL = fmaf(ex2(ml0[0] - MM), ml0[1], ex2(ml1[0] - MM) * ml1[1]);
      __builtin_amdgcn_raw_buffer_store_b128(u32x4{__float_as_uint(MM), tag1, __float_as_uint(LL), tag1}, w1r, lane == 0 ? 768 : OOB, SLOT0 + slot1 * SLOT, SA);
      STAMP(0);
      if (PM >> 8) __builtin_amdgcn_s_sleep(PM >> 8);
      else {
#pragma nounroll
        for (unsigned spin = 0; spin < (1u << 24); ++spin) { if (ctl[10] >= ((PM & 2) ? 1u : 2u)) break; }
      }
    }
    bool timeout = false;
    float wa1, wb1, t1x, t1y;
    u32x4 xa, xb; unsigned o0;
    auto fin0 = [&]() {
      __builtin_amdgcn_raw_buffer_store_b32(o0, o0r, lane < 8 ? dim0 * 2 : OOB, 0, 0);
      __builtin_amdgcn_raw_buffer_store_b32(tag0, w0r, (slot0 == 0u && lane == 0) ? 0 : OOB, 0, (PM & 128) ? 0 : 16);
      __builtin_amdgcn_raw_buffer_store_b32(tag1, w1r, (slot1 == 0u && lane == 0) ? 0 : OOB, 0, (PM & 128) ? 0 : 16);
    };
    STAMP(4);
    {
      u32x4 va, vb, ma, mb, na, nb;
#pragma nounroll
      for (unsigned spin = 0;; ++spin) {
        asm volatile("" ::: "memory");
        va = __builtin_amdgcn_raw_buffer_load_b128(w0r, sa0 + in0, 0, LA); vb = __builtin_amdgcn_raw_buffer_load_b128(w0r, sb0 + in0, 0, LA);
        ma = __builtin_amdgcn_raw_buffer_load_b128(w0r, sa0 + 768, 0, LA); mb = __builtin_amdgcn_raw_buffer_load_b128(w0r, sb0 + 768, 0, LA);
        na = __builtin_amdgcn_raw_buffer_load_b128(w1r, sa1 + 768, 0, LA); nb = __builtin_amdgcn_raw_buffer_load_b128(w1r, sb1 + 768, 0, LA);
        const unsigned bad = (va[1] ^ tag0) | (va[3] ^ tag0) | (vb[1] ^ tag0) | (vb[3] ^ tag0) | (ma[1] ^ tag0) | (ma[3] ^ tag0) | (mb[1] ^ tag0) | (mb[3] ^ tag0)
                           | (na[1] ^ tag1) | (na[3] ^ tag1) | (nb[1] ^ tag1) | (nb[3] ^ tag1);
        if constexpr (DBG) ++n1_;
        if (__all(bad == 0u) || pass == 0u) break;
        if (spin > (1u << 20)) { timeout = true; break; }
      }
      STAMP(3);
      if ((PM >> 8) && pass) {
#pragma nounroll
        for (unsigned spin = 0; spin < (1u << 24); ++spin) { if (ctl[10] >= 2u) break; }
      }
      asm volatile("" ::: "memory");
      xa = __builtin_amdgcn_raw_buffer_load_b128(w1r, sa1 + in1, 0, LA); xb = __builtin_amdgcn_raw_buffer_load_b128(w1r, sb1 + in1, 0, LA);
      asm volatile("" ::: "memory");
      __builtin_amdgcn_sched_barrier(0);
      {
        const float M = fmaxf(xmax(fmaxf(__uint_as_float(ma[0]), __uint_as_float(mb[0]))), sct0);
        const float wa = ex2(__uint_as_float(ma[0]) - M), wb = vb0 ? ex2(__uint_as_float(mb[0]) - M) : 0.f, wt = ex2(sct0 - M);
        const float L = xsum(fmaf(wa, __uint_as_float(ma[2]), wb * __uint_as_float(mb[2]))) + wt;
        const float x = xsum(fmaf(wa, __uint_as_float(va[0]), wb * __uint_as_float(vb[0]))), y = xsum(fmaf(wa, __uint_as_float(va[2]), wb * __uint_as_float(vb[2])));
        const float inv = timeout ? __builtin_nanf("") : __builtin_amdgcn_rcpf(L);
        o0 = __builtin_bit_cast(unsigned, bf16x2{(__bf16)(fmaf(wt, bflo(vt0), x) * inv), (__bf16)(fmaf(wt, bfhi(vt0), y) * inv)});
        if (!(PM & 64)) fin0();
      }
      {
        const float M = fmaxf(xmax(fmaxf(__uint_as_float(na[0]), __uint_as_float(nb[0]))), sct1);
        const float wa = ex2(__uint_as_float(na[0]) - M), wb = vb1 ? ex2(__uint_as_float(nb[0]) - M) : 0.f, wt = ex2(sct1 - M);
        const float inv = __builtin_amdgcn_rcpf(0.5f * xsum(fmaf(wa, __uint_as_float(na[2]), wb * __uint_as_float(nb[2]))) + wt);
        wa1 = wa * inv; wb1 = wb * inv; t1x = wt * inv * bflo(vt1); t1y = wt * inv * bfhi(vt1);
      }
    }
    asm volatile("" : "+v"(wa1), "+v"(wb1), "+v"(t1x), "+v"(t1y), "+v"(o0));
    STAMP(1);
    {
      u32x4 va = xa, vb = xb;
#pragma nounroll
      for (unsigned spin = 0;; ++spin) {
        unsigned bad = (va[1] ^ tag1) | (va[3] ^ tag1) | (vb[1] ^ tag1) | (vb[3] ^ tag1);
        if constexpr (DBG) { asm volatile("" : "+v"(bad)); if (nit_ == 0) STAMP(2); nit_ += pass; }
        if (__all(bad == 0u) || pass == 0u) break;
        if (spin > (1u << 20)) { timeout = true; break; }
        asm volatile("" ::: "memory");
        va = __builtin_amdgcn_raw_buffer_load_b128(w1r, sa1 + in1, 0, LA); vb = __builtin_amdgcn_raw_buffer_load_b128(w1r, sb1 + in1, 0, LA);
      }
      float x = fmaf(wa1, __uint_as_float(va[0]), wb1 * __uint_as_float(vb[0])), y = fmaf(wa1, __uint_as_float(va[2]), wb1 * __uint_as_float(vb[2]));
      { auto s1 = __builtin_amdgcn_permlane32_swap(__float_as_uint(x), __float_as_uint(y), false, false);
        float z = __uint_as_float(s1[0]) + __uint_as_float(s1[1]);
        auto s2 = __builtin_amdgcn_permlane16_swap(__float_as_uint(z), __float_as_uint(z), false, false);
        z = __uint_as_float(s2[0]) + __uint_as_float(s2[1]);
        auto s3 = __builtin_amdgcn_permlane32_swap(__float_as_uint(z), __float_as_uint(z), false, false);
        x = __uint_as_float(s3[0]) + t1x; y = __uint_as_float(s3[1]) + t1y; }
      if (timeout) x = __builtin_nanf("");
      __builtin_amdgcn_raw_buffer_store_b32(__builtin_bit_cast(unsigned, bf16x2{(__bf16)x, (__bf16)y}), o1r, lane < 16 ? dim1 * 2 : OOB, 0, 0);
      if (PM & 64) fin0();
    }
  }
  if constexpr (DBG) { STAMP(5); if (lane == 0) { unsigned long long* sp = (unsigned long long*)(ws + (64 << 20)) + (size_t)(blk * W) * 8; sp[4] = ts[4]; sp[5] = ts[5]; sp[8 + 4] = ts[4]; sp[8 + 5] = ts[5];
    unsigned long long* hp = (unsigned long long*)(ws + (80 << 20)) + (size_t)blk * 8; hp[0] = ts[4]; hp[1] = ts[0]; hp[7] = ts[3]; hp[2] = ts[2]; hp[3] = ts[5]; hp[4] = nit_; hp[5] = h0 == 42u ? 1 : (h1 == 42u ? 2 : 0); hp[6] = h1 | (h0 << 8) | ((unsigned long long)n1_ << 16); } }
}
}

extern "C" void kernel_launch(void* const* d_in, const int* in_sizes, int n_in, void* d_out, int out_size, void* d_ws, size_t ws_size,
                              hipStream_t stream) {
  th::attn9<8,9,12,30,162,16,512,50,66,0><<<256, 192, 0, stream>>>((const unsigned char*)d_in[0], (const unsigned char*)d_in[1], (const unsigned char*)d_in[2], (const unsigned char*)d_in[3],
      (const unsigned char*)d_in[4], (const unsigned short*)d_in[5], (unsigned short*)d_out, (unsigned char*)d_ws);
}
```
